```python
import torch

def _softmax(x, axis=-1):
    return torch.softmax(x, dim=axis)
def _gelu(x):
    return torch.nn.functional.gelu(x, approximate="tanh")
def _silu(x):
    return torch.nn.functional.silu(x)
def _sigmoid(x):
    return torch.sigmoid(x)
def _layernorm(x, axis=-1, eps=1e-5):
    m = x.mean(dim=axis, keepdim=True)
    v = x.var(dim=axis, keepdim=True, unbiased=False)
    return (x - m) / torch.sqrt(v + eps)
def _scatter_add(tgt, idx, src, axis=0):
    return tgt.clone().index_add_(axis, idx, src)
def _segment_sum(src, seg, n):
    out = torch.zeros((n,) + src.shape[1:], dtype=src.dtype)
    return out.index_add_(0, seg, src)
def _pad(x, pw):
    flat = tuple(v for pair in reversed(pw) for v in pair)
    return torch.nn.functional.pad(x, flat)

kernel_name = 'gated_mix__grouped_kv_mqa__glu_gated_bilinear__prompt'

def setup_inputs():

    _w = (lambda a: (lambda t: t.to(torch.bfloat16) if t.is_floating_point() else t)(torch.as_tensor(a)))
    def _A_setup():
        B, S, D, H = 1, 2048, 448, 16
        Dh = D // H
        return {
            "x": _w((lambda *s: torch.randn(*s, dtype=torch.bfloat16) * 0.05)(B, S, D)),
            "wq": _w((lambda *s: torch.randn(*s, dtype=torch.bfloat16) * 0.8)(D, H * Dh)),
            "wk": _w((lambda *s: torch.randn(*s, dtype=torch.bfloat16) * 0.8)(D, Dh)),
            "wv": _w((lambda *s: torch.randn(*s, dtype=torch.bfloat16) * 0.05)(D, Dh)),
            "wo": _w((lambda *s: torch.randn(*s, dtype=torch.bfloat16) * 0.05)(H * Dh, D)),
        }
    _A_in = _A_setup()

    def _B_setup():
        B, S, D, F = 1, 2048, 448, 1792
        return {
            "x": _w((lambda *s: torch.randn(*s, dtype=torch.bfloat16) * 0.05)(B, S, D)),
            "w_gate": _w((lambda *s: torch.randn(*s, dtype=torch.bfloat16) * 0.8)(D, F) * (D ** -0.5)),
            "w_up": _w((lambda *s: torch.randn(*s, dtype=torch.bfloat16) * 0.8)(D, F) * (D ** -0.5)),
            "w_down": _w((lambda *s: torch.randn(*s, dtype=torch.bfloat16) * 0.8)(F, D) * (F ** -0.5)),
        }
    _B_in = _B_setup()

    return {
        "A_x": _A_in["x"],
        "A_wq": _A_in["wq"],
        "A_wk": _A_in["wk"],
        "A_wv": _A_in["wv"],
        "A_wo": _A_in["wo"],
        "B_x": _B_in["x"],
        "B_w_gate": _B_in["w_gate"],
        "B_w_up": _B_in["w_up"],
        "B_w_down": _B_in["w_down"],
        "gate_w": _w((lambda *s: torch.randn(*s, dtype=torch.bfloat16) * 0.05)(448) * 16.0),
    }

def reference(A_x, A_wq, A_wk, A_wv, A_wo, B_x, B_w_gate, B_w_up, B_w_down, gate_w):
    def _A_ref(x, wq, wk, wv, wo):
        B, S, D = x.shape
        H = 16
        Dh = D // H
        q = (lambda x, *d: x.permute(*d))((x @ wq).reshape(B, S, H, Dh), 0, 2, 1, 3)
        k = x @ wk
        v = x @ wv
        scores = torch.einsum("bhqd,bkd->bhqk", q, k) * (Dh ** -0.5)
        p = _softmax(scores, axis=-1)
        o = torch.einsum("bhqk,bkd->bhqd", p, v)
        return (lambda x, *d: x.permute(*d))(o, 0, 2, 1, 3).reshape(B, S, H * Dh) @ wo

    def _B_ref(x, w_gate, w_up, w_down):
        g = torch.einsum("bsd,df->bsf", x, w_gate)
        u = torch.einsum("bsd,df->bsf", x, w_up)
        h = g * u
        return torch.einsum("bsf,fd->bsd", h, w_down)

    x = A_x
    g = _sigmoid((x * gate_w).sum(-1, keepdims=True))
    a = _A_ref(x, A_wq, A_wk, A_wv, A_wo)
    b = _B_ref(x, B_w_gate, B_w_up, B_w_down)
    return g * a + (1 - g) * b
```

```cpp
#include <hip/hip_runtime.h>
#include <hip/hip_bf16.h>
#include <stdint.h>

typedef __attribute__((ext_vector_type(8))) short bf16x8;
typedef __attribute__((ext_vector_type(4))) short s16x4;
typedef __attribute__((ext_vector_type(16))) float f32x16;
typedef __attribute__((ext_vector_type(4))) float f32x4;
typedef __attribute__((ext_vector_type(4))) unsigned u32x4;
typedef __attribute__((ext_vector_type(2))) unsigned u32x2;
#define LAS __attribute__((address_space(3)))
#define MFMA(a, b, c) __builtin_amdgcn_mfma_f32_32x32x16_bf16(a, b, c, 0, 0, 0)
#define SBAR() __builtin_amdgcn_sched_barrier(0)
#define PIN(x) asm volatile("" : "+v"(x))

constexpr int S_ = 2048, D_ = 448, F_ = 1792, DH = 28;
constexpr size_t WS_Q = 0;
constexpr size_t WS_KV = WS_Q + (2u << 20);
constexpr size_t WS_H = WS_KV + (256u << 10);
constexpr size_t WS_WD = WS_H + 64 * 112 * 1024;
constexpr size_t WS_WO = WS_WD + 14 * 112 * 1024;
constexpr size_t WS_O = WS_WO + 14 * 32 * 1024;
constexpr size_t WS_B = WS_O + (2u << 20);
constexpr size_t WS_G = WS_B + 64 * 14 * 4096;

__device__ __forceinline__ unsigned cvtpk(float lo, float hi) {
  unsigned r; asm("v_cvt_pk_bf16_f32 %0, %1, %2" : "=v"(r) : "v"(lo), "v"(hi)); return r;
}
__device__ __forceinline__ float bf2f(unsigned short b) { return __uint_as_float(((unsigned)b) << 16); }
__device__ __forceinline__ float rbf(float x) { return __uint_as_float(cvtpk(x, 0.f) << 16); }
__device__ __forceinline__ void glds16(const void* g, LAS void* l) {
  __builtin_amdgcn_global_load_lds((const unsigned*)g, (LAS unsigned*)l, 16, 0, 0);
}

constexpr int K1_STAGE = 49152, K1_LDS = 3 * K1_STAGE;
__global__ __launch_bounds__(512, 2) void k1_proj(
    const unsigned short* __restrict__ x, const unsigned short* __restrict__ wq, const unsigned short* __restrict__ wk,
    const unsigned short* __restrict__ wv, const unsigned short* __restrict__ wo, const unsigned short* __restrict__ wg,
    const unsigned short* __restrict__ wu, const unsigned short* __restrict__ wd, const unsigned short* __restrict__ gw,
    char* __restrict__ ws) {
  extern __shared__ __attribute__((aligned(16))) char smem[];
  const int tid = threadIdx.x, lane = tid & 63, w = __builtin_amdgcn_readfirstlane(tid >> 6);
  const int hh = lane >> 5, l31 = lane & 31;
  const int bid = blockIdx.x, xcd = bid & 7, idx = bid >> 3;
  const int rb = (xcd & 1) * 8 + (idx & 7), cg = (xcd >> 1) * 4 + (idx >> 3);
  const int m0 = rb * 128;

  const int gwave = bid * 8 + w;
  u32x4 gx = {0, 0, 0, 0}, gv = {0, 0, 0, 0};
  if (lane < 56) { gx = *(const u32x4*)(x + (size_t)gwave * D_ + lane * 8); gv = *(const u32x4*)(gw + lane * 8); }
  unsigned short pt[8];
  {
    const unsigned short* src = wd; int rstride = D_; bool valid[8];
    if (gwave < 1568) { const int nt = gwave / 112, fs = gwave % 112;
      #pragma unroll
      for (int j = 0; j < 8; j++) { const int f = 16 * fs + 8 * (j >> 2) + 4 * hh + (j & 3); pt[j] = wd[(size_t)f * D_ + 32 * nt + l31]; }
    } else if (gwave < 2016) { const int e = gwave - 1568, nt = e >> 5, head = (e >> 1) & 15, s = e & 1;
      #pragma unroll
      for (int j = 0; j < 8; j++) { const int r = 16 * s + 8 * (j >> 2) + 4 * hh + (j & 3);
        const bool pad = (r >= 24 && r < 28); const int d = r < 24 ? r : r - 4;
        unsigned short v = wo[(size_t)(head * DH + (pad ? 0 : d)) * D_ + 32 * nt + l31]; pt[j] = pad ? (unsigned short)0 : v; }
    } else {
      #pragma unroll
      for (int j = 0; j < 8; j++) pt[j] = 0;
    }
    (void)src; (void)rstride; (void)valid;
  }

  const char* wp[4]; int wstride;
  {
    const int krow = 8 * w + ((lane >> 2) & 7), ch = lane & 3, cl = lane >> 5;
    if (cg < 14) {
      wp[0] = (const char*)(wg + (size_t)krow * F_ + 128 * cg + 32 * (0 + cl) + 8 * ch);
      wp[1] = (const char*)(wg + (size_t)krow * F_ + 128 * cg + 32 * (2 + cl) + 8 * ch);
      wp[2] = (const char*)(wu + (size_t)krow * F_ + 128 * cg + 32 * (0 + cl) + 8 * ch);
      wp[3] = (const char*)(wu + (size_t)krow * F_ + 128 * cg + 32 * (2 + cl) + 8 * ch);
      wstride = 64 * F_ * 2;
    } else {
      const int cb = (cg - 14) * 256;
      wp[0] = (const char*)(wq + (size_t)krow * D_ + cb + 32 * (0 + cl) + 8 * ch);
      wp[1] = (const char*)(wq + (size_t)krow * D_ + cb + 32 * (2 + cl) + 8 * ch);
      wp[2] = (const char*)(wq + (size_t)krow * D_ + cb + 32 * (4 + cl) + 8 * ch);
      if (cg == 14) wp[3] = (const char*)(wq + (size_t)krow * D_ + cb + 32 * (6 + cl) + 8 * ch);
      else wp[3] = (const char*)((cl ? wv : wk) + (size_t)krow * DH + (ch == 3 ? 20 : 8 * ch));
      wstride = 64 * D_ * 2;
    }
  }
  const int wstride3 = (cg == 15) ? 64 * DH * 2 : wstride;
  const char* xp[2];
  #pragma unroll
  for (int i = 0; i < 2; i++) { const int m = 8 * (2 * w + i) + (lane >> 3); const int ch = (lane & 7) ^ ((m >> 1) & 7);
    xp[i] = (const char*)(x + (size_t)(m0 + m) * D_ + 8 * ch); }

  auto stage_issue = [&](int st, int buf) {
    char* wb = smem + buf * K1_STAGE; char* xb = wb + 32768;
    #pragma unroll
    for (int ii = 0; ii < 4; ii++) glds16(wp[ii] + (size_t)st * (ii == 3 ? wstride3 : wstride), (LAS void*)(wb + (4 * w + ii) * 1024));
    #pragma unroll
    for (int i = 0; i < 2; i++) glds16(xp[i] + st * 128, (LAS void*)(xb + (2 * w + i) * 1024));
  };
  stage_issue(0, 0); stage_issue(1, 1); stage_issue(2, 2);

  const int wf = w & 3, wm = w >> 2;
  const int abase = 4096 * hh + 64 * ((lane & 15) >> 2) + 32 * ((lane >> 4) & 1) + 8 * (lane & 3) + 512 * wf;
  int bbase[4];
  #pragma unroll
  for (int ks = 0; ks < 4; ks++) bbase[ks] = 32768 + 128 * (64 * wm + l31) + 16 * ((2 * ks + hh) ^ ((lane >> 1) & 7));
  f32x16 acc[2][2];
  #pragma unroll
  for (int a = 0; a < 2; a++) for (int b = 0; b < 2; b++) for (int r = 0; r < 16; r++) acc[a][b][r] = 0.f;

  #pragma unroll
  for (int st = 0; st < 7; st++) {
    const int buf = st % 3;
    if (st <= 4) { if (st == 0) asm volatile("s_waitcnt vmcnt(12)" ::: "memory"); else asm volatile("s_waitcnt vmcnt(6)" ::: "memory"); }
    else if (st == 5) asm volatile("s_waitcnt vmcnt(6)" ::: "memory");
    else asm volatile("s_waitcnt vmcnt(0)" ::: "memory");
    asm volatile("s_waitcnt lgkmcnt(0)\n\ts_barrier" ::: "memory");
    if (st >= 1 && st + 2 < 7) stage_issue(st + 2, (st + 2) % 3);
    const LAS char* sb = (const LAS char*)(smem + buf * K1_STAGE);
    #pragma unroll
    for (int ks = 0; ks < 4; ks++) {
      bf16x8 af[2], bfr[2];
      #pragma unroll
      for (int a = 0; a < 2; a++) {
        s16x4 lo = __builtin_amdgcn_ds_read_tr16_b64_v4i16((LAS s16x4*)(sb + abase + 8192 * ks + 2048 * a));
        s16x4 hi = __builtin_amdgcn_ds_read_tr16_b64_v4i16((LAS s16x4*)(sb + abase + 8192 * ks + 2048 * a + 256));
        af[a] = (bf16x8){lo[0], lo[1], lo[2], lo[3], hi[0], hi[1], hi[2], hi[3]};
      }
      #pragma unroll
      for (int b = 0; b < 2; b++) bfr[b] = *(const LAS bf16x8*)(sb + bbase[ks] + 4096 * b);
      #pragma unroll
      for (int a = 0; a < 2; a++)
        #pragma unroll
        for (int b = 0; b < 2; b++) acc[a][b] = MFMA(af[a], bfr[b], acc[a][b]);
    }
  }

  if (cg < 14) {
    #pragma unroll
    for (int b = 0; b < 2; b++) {
      const int tt = 4 * rb + 2 * wm + b, ft = 4 * cg + wf;
      #pragma unroll
      for (int s = 0; s < 2; s++) {
        u32x4 o;
        #pragma unroll
        for (int j = 0; j < 4; j++) {
          const int r = 8 * s + 2 * j;
          o[j] = cvtpk(rbf(acc[0][b][r]) * rbf(acc[1][b][r]), rbf(acc[0][b][r + 1]) * rbf(acc[1][b][r + 1]));
        }
        *(u32x4*)(ws + WS_H + ((size_t)(tt * 112 + 2 * ft + s) * 64 + lane) * 16) = o;
      }
    }
  } else {
    const float qs = 0.18898223650461361f * 1.4426950408889634f;
    #pragma unroll
    for (int a = 0; a < 2; a++) {
      const int c = wf + 4 * a;
      const int cglob = (cg - 14) * 8 + c;
      #pragma unroll
      for (int b = 0; b < 2; b++) {
        const int tt = 4 * rb + 2 * wm + b;
        if (cglob < 14) {
          #pragma unroll
          for (int g = 0; g < 4; g++) {
            const int n0 = 32 * cglob + 8 * g + 4 * hh, head = n0 / 28, u = (n0 - head * 28) >> 2;
            const int pos = (u == 6) ? 7 : u;
            char* base = ws + WS_Q + (size_t)(tt * 16 + head) * 2048;
            u32x2 v = {cvtpk(acc[a][b][4 * g] * qs, acc[a][b][4 * g + 1] * qs), cvtpk(acc[a][b][4 * g + 2] * qs, acc[a][b][4 * g + 3] * qs)};
            *(u32x2*)(base + (pos >> 2) * 1024 + ((pos & 1) * 32 + l31) * 16 + ((pos >> 1) & 1) * 8) = v;
            if (u == 6) *(u32x2*)(base + 1024 + l31 * 16 + 8) = (u32x2){0u, 0u};
          }
        } else if (cglob == 14) {
          #pragma unroll
          for (int s = 0; s < 2; s++) {
            u32x4 o;
            #pragma unroll
            for (int j = 0; j < 4; j++) o[j] = cvtpk(acc[a][b][8 * s + 2 * j], acc[a][b][8 * s + 2 * j + 1]);
            if (s == 1 && hh == 0) { o[2] = 0u; o[3] = 0u; }
            *(u32x4*)(ws + WS_KV + (size_t)tt * 4096 + s * 1024 + lane * 16) = o;
          }
        } else {
          char* base = ws + WS_KV + (size_t)tt * 4096 + 2048 + (l31 >> 4) * 1024 + ((l31 >> 2) & 1) * 512 + (4 * ((l31 >> 3) & 1) + (l31 & 3)) * 2;
          #pragma unroll
          for (int r16 = 0; r16 < 16; r16++) {
            const int r = (r16 & 3) + 8 * (r16 >> 2) + 4 * hh;
            unsigned short v = (unsigned short)(cvtpk(acc[a][b][r16], 0.f) & 0xffffu);
            if (r16 >= 12 && hh == 0) v = (r16 == 12) ? (unsigned short)0x3f80 : (unsigned short)0;
            *(unsigned short*)(base + r * 16) = v;
          }
        }
      }
    }
  }
  if (gwave < 2016) {
    u32x4 o = {(unsigned)pt[0] | ((unsigned)pt[1] << 16), (unsigned)pt[2] | ((unsigned)pt[3] << 16),
               (unsigned)pt[4] | ((unsigned)pt[5] << 16), (unsigned)pt[6] | ((unsigned)pt[7] << 16)};
    char* dst = (gwave < 1568) ? ws + WS_WD + (size_t)gwave * 1024 : ws + WS_WO + (size_t)(gwave - 1568) * 1024;
    *(u32x4*)(dst + lane * 16) = o;
  }
  {
    float sum = 0.f;
    #pragma unroll
    for (int i = 0; i < 4; i++) {
      sum += rbf(__uint_as_float(gx[i] << 16) * __uint_as_float(gv[i] << 16));
      sum += rbf(__uint_as_float(gx[i] & 0xffff0000u) * __uint_as_float(gv[i] & 0xffff0000u));
    }
    #pragma unroll
    for (int o = 32; o >= 1; o >>= 1) sum += __shfl_xor(sum, o);
    if (lane == 0) { const float z = rbf(sum); ((float*)(ws + WS_G))[gwave] = rbf(1.f / (1.f + __expf(-z))); }
  }
}

template <int NSTEP, int DEPTH>
__device__ __forceinline__ void gemm_stream(const char* A0, const char* A1, const char* B0, const char* B1, f32x16 (&acc)[2][2]) {
  bf16x8 ra[DEPTH][2], rb[DEPTH][2];
  #pragma unroll
  for (int i = 0; i < DEPTH; i++) {
    ra[i][0] = *(const bf16x8*)(A0 + i * 1024); ra[i][1] = *(const bf16x8*)(A1 + i * 1024);
    rb[i][0] = *(const bf16x8*)(B0 + i * 1024); rb[i][1] = *(const bf16x8*)(B1 + i * 1024);
  }
  #pragma unroll
  for (int i = 0; i < NSTEP; i++) {
    const int sl = i % DEPTH;
    bf16x8 a0 = ra[sl][0], a1 = ra[sl][1], b0 = rb[sl][0], b1 = rb[sl][1];
    acc[0][0] = MFMA(a0, b0, acc[0][0]); acc[0][1] = MFMA(a0, b1, acc[0][1]);
    acc[1][0] = MFMA(a1, b0, acc[1][0]); acc[1][1] = MFMA(a1, b1, acc[1][1]);
    if (i + DEPTH < NSTEP) {
      ra[sl][0] = *(const bf16x8*)(A0 + (i + DEPTH) * 1024); ra[sl][1] = *(const bf16x8*)(A1 + (i + DEPTH) * 1024);
      rb[sl][0] = *(const bf16x8*)(B0 + (i + DEPTH) * 1024); rb[sl][1] = *(const bf16x8*)(B1 + (i + DEPTH) * 1024);
    }
  }
}
__device__ __forceinline__ void reduce4_write(char* red, int kq, int lane, f32x16 (&acc)[2][2]) {
  #pragma unroll
  for (int t = 0; t < 4; t++) {
    if (t != kq) {
      const int slot = (kq < t) ? kq : kq - 1;
      #pragma unroll
      for (int g = 0; g < 4; g++) {
        f32x4 v = {acc[t >> 1][t & 1][4 * g], acc[t >> 1][t & 1][4 * g + 1], acc[t >> 1][t & 1][4 * g + 2], acc[t >> 1][t & 1][4 * g + 3]};
        *(f32x4*)(red + (t * 3 + slot) * 4096 + g * 1024 + lane * 16) = v;
      }
    }
  }
}
__device__ __forceinline__ f32x16 reduce4_read(const char* red, int kq, int lane, f32x16 (&acc)[2][2]) {
  f32x16 o;
  if (kq == 0) o = acc[0][0]; else if (kq == 1) o = acc[0][1]; else if (kq == 2) o = acc[1][0]; else o = acc[1][1];
  #pragma unroll
  for (int sl = 0; sl < 3; sl++)
    #pragma unroll
    for (int g = 0; g < 4; g++) {
      f32x4 v = *(const f32x4*)(red + (kq * 3 + sl) * 4096 + g * 1024 + lane * 16);
      o[4 * g] += v[0]; o[4 * g + 1] += v[1]; o[4 * g + 2] += v[2]; o[4 * g + 3] += v[3];
    }
  return o;
}

constexpr int K2_STAGE = 32768, K2_NBUF = 3, K2_LDS = K2_NBUF * K2_STAGE;
__global__ __launch_bounds__(512, 2) void k2_attn(char* __restrict__ ws) {
  extern __shared__ __attribute__((aligned(16))) char smem[];
  const int tid = threadIdx.x, lane = tid & 63, w = __builtin_amdgcn_readfirstlane(tid >> 6);
  const int hh = lane >> 5;
  const int qt = blockIdx.x * 4 + (w & 3), half = w >> 2;
  const char* kv = ws + WS_KV;
  auto issue = [&](int st, int buf) {
    const char* src = kv + (size_t)((half * 32 + 4 * st + (w & 3)) * 4096) + lane * 16;
    char* dst = smem + buf * K2_STAGE + w * 4096;
    #pragma unroll
    for (int i = 0; i < 4; i++) glds16(src + i * 1024, (LAS void*)(dst + i * 1024));
  };
  bf16x8 q0 = *(const bf16x8*)(ws + WS_Q + (size_t)qt * 2048 + lane * 16);
  bf16x8 q1 = *(const bf16x8*)(ws + WS_Q + (size_t)qt * 2048 + 1024 + lane * 16);
  PIN(q0); PIN(q1);
  issue(0, 0); issue(1, 1);
  f32x16 O;
  #pragma unroll
  for (int r = 0; r < 16; r++) O[r] = 0.f;
  const f32x16 Z = {0, 0, 0, 0, 0, 0, 0, 0, 0, 0, 0, 0, 0, 0, 0, 0};
  for (int st = 0; st < 8; st++) {
    const int buf = st % 3;
    if (st < 7) asm volatile("s_waitcnt vmcnt(4) lgkmcnt(0)\n\ts_barrier" ::: "memory");
    else asm volatile("s_waitcnt vmcnt(0) lgkmcnt(0)\n\ts_barrier" ::: "memory");
    if (st + 2 < 8) issue(st + 2, (st + 2) % 3);
    const LAS char* sb = (const LAS char*)(smem + buf * K2_STAGE + half * 16384 + lane * 16);
    #pragma unroll
    for (int t = 0; t < 4; t++) {
      bf16x8 k0 = *(const LAS bf16x8*)(sb + t * 4096), k1 = *(const LAS bf16x8*)(sb + t * 4096 + 1024);
      bf16x8 v0 = *(const LAS bf16x8*)(sb + t * 4096 + 2048), v1 = *(const LAS bf16x8*)(sb + t * 4096 + 3072);
      f32x16 Sx = MFMA(k0, q0, Z); Sx = MFMA(k1, q1, Sx);
      u32x4 p0, p1;
      #pragma unroll
      for (int r = 0; r < 16; r++) Sx[r] = __builtin_amdgcn_exp2f(Sx[r]);
      #pragma unroll
      for (int j = 0; j < 4; j++) {
        p0[j] = __builtin_amdgcn_perm(__float_as_uint(Sx[2 * j + 1]), __float_as_uint(Sx[2 * j]), 0x07060302u);
        p1[j] = __builtin_amdgcn_perm(__float_as_uint(Sx[8 + 2 * j + 1]), __float_as_uint(Sx[8 + 2 * j]), 0x07060302u);
      }
      O = MFMA(v0, __builtin_bit_cast(bf16x8, p0), O); O = MFMA(v1, __builtin_bit_cast(bf16x8, p1), O);
    }
  }
  asm volatile("s_waitcnt lgkmcnt(0)\n\ts_barrier" ::: "memory");
  if (half == 1) {
    #pragma unroll
    for (int g = 0; g < 4; g++) *(f32x4*)(smem + (w & 3) * 4096 + g * 1024 + lane * 16) = (f32x4){O[4 * g], O[4 * g + 1], O[4 * g + 2], O[4 * g + 3]};
  }
  __syncthreads();
  if (half == 0) {
    #pragma unroll
    for (int g = 0; g < 4; g++) { f32x4 v = *(const f32x4*)(smem + (w & 3) * 4096 + g * 1024 + lane * 16);
      O[4 * g] += v[0]; O[4 * g + 1] += v[1]; O[4 * g + 2] += v[2]; O[4 * g + 3] += v[3]; }
    const float l = __shfl(O[12], lane & 31);
    const float inv = 1.f / l;
    u32x4 o0, o1;
    #pragma unroll
    for (int j = 0; j < 4; j++) { o0[j] = cvtpk(O[2 * j] * inv, O[2 * j + 1] * inv); o1[j] = cvtpk(O[8 + 2 * j] * inv, O[8 + 2 * j + 1] * inv); }
    if (hh == 0) { o1[2] = 0u; o1[3] = 0u; }
    *(u32x4*)(ws + WS_O + (size_t)qt * 2048 + lane * 16) = o0;
    *(u32x4*)(ws + WS_O + (size_t)qt * 2048 + 1024 + lane * 16) = o1;
  }
}

__global__ __launch_bounds__(256, 2) void k2_down(char* __restrict__ ws) {
  extern __shared__ __attribute__((aligned(16))) char smem[];
  const int tid = threadIdx.x, lane = tid & 63, kq = __builtin_amdgcn_readfirstlane(tid >> 6);
  const int bid = blockIdx.x, xcd = bid & 7, idx = bid >> 3;
  const int tp = xcd * 4 + (idx & 3), np = idx >> 2;
  f32x16 acc[2][2];
  #pragma unroll
  for (int a = 0; a < 2; a++) for (int b = 0; b < 2; b++) for (int r = 0; r < 16; r++) acc[a][b][r] = 0.f;
  const char* A0 = ws + WS_WD + (size_t)((2 * np) * 112 + 28 * kq) * 1024 + lane * 16;
  const char* B0 = ws + WS_H + (size_t)((2 * tp) * 112 + 28 * kq) * 1024 + lane * 16;
  gemm_stream<28, 4>(A0, A0 + 112 * 1024, B0, B0 + 112 * 1024, acc);
  reduce4_write(smem, kq, lane, acc);
  __syncthreads();
  f32x16 o = reduce4_read(smem, kq, lane, acc);
  const int nt = 2 * np + (kq >> 1), tt = 2 * tp + (kq & 1);
  #pragma unroll
  for (int g = 0; g < 4; g++)
    *(f32x4*)(ws + WS_B + (size_t)(tt * 14 + nt) * 4096 + g * 1024 + lane * 16) = (f32x4){o[4 * g], o[4 * g + 1], o[4 * g + 2], o[4 * g + 3]};
}

__global__ __launch_bounds__(256, 2) void k3_out(const char* __restrict__ ws, unsigned short* __restrict__ out) {
  extern __shared__ __attribute__((aligned(16))) char smem[];
  const int tid = threadIdx.x, lane = tid & 63, kq = __builtin_amdgcn_readfirstlane(tid >> 6);
  const int hh = lane >> 5, l31 = lane & 31;
  const int bid = blockIdx.x, xcd = bid & 7, idx = bid >> 3;
  const int tp = xcd * 4 + (idx & 3), np = idx >> 2;
  const int nt = 2 * np + (kq >> 1), tt = 2 * tp + (kq & 1);
  f32x4 bb[4];
  #pragma unroll
  for (int g = 0; g < 4; g++) bb[g] = *(const f32x4*)(ws + WS_B + (size_t)(tt * 14 + nt) * 4096 + g * 1024 + lane * 16);
  const float gt = ((const float*)(ws + WS_G))[tt * 32 + l31];
  f32x16 acc[2][2];
  #pragma unroll
  for (int a = 0; a < 2; a++) for (int b = 0; b < 2; b++) for (int r = 0; r < 16; r++) acc[a][b][r] = 0.f;
  const char* A0 = ws + WS_WO + (size_t)((2 * np) * 32 + 8 * kq) * 1024 + lane * 16;
  const char* B0 = ws + WS_O + (size_t)((2 * tp) * 32 + 8 * kq) * 1024 + lane * 16;
  gemm_stream<8, 8>(A0, A0 + 32 * 1024, B0, B0 + 32 * 1024, acc);
  reduce4_write(smem, kq, lane, acc);
  __syncthreads();
  f32x16 o = reduce4_read(smem, kq, lane, acc);
  const float og = rbf(1.f - gt);
  unsigned short* orow = out + (size_t)(tt * 32 + l31) * D_ + nt * 32 + 4 * hh;
  #pragma unroll
  for (int g = 0; g < 4; g++) {
    float v[4];
    #pragma unroll
    for (int e = 0; e < 4; e++) v[e] = rbf(gt * rbf(o[4 * g + e])) + rbf(og * rbf(bb[g][e]));
    *(u32x2*)(orow + 8 * g) = (u32x2){cvtpk(v[0], v[1]), cvtpk(v[2], v[3])};
  }
}

extern "C" void kernel_launch(void* const* d_in, const int* in_sizes, int n_in, void* d_out, int out_size, void* d_ws, size_t ws_size, hipStream_t stream) {
  const unsigned short* x = (const unsigned short*)d_in[0];
  const unsigned short* wq = (const unsigned short*)d_in[1];
  const unsigned short* wk = (const unsigned short*)d_in[2];
  const unsigned short* wv = (const unsigned short*)d_in[3];
  const unsigned short* wo = (const unsigned short*)d_in[4];
  const unsigned short* wg = (const unsigned short*)d_in[6];
  const unsigned short* wu = (const unsigned short*)d_in[7];
  const unsigned short* wd = (const unsigned short*)d_in[8];
  const unsigned short* gw = (const unsigned short*)d_in[9];
  char* ws = (char*)d_ws;
  hipFuncSetAttribute((const void*)k1_proj, hipFuncAttributeMaxDynamicSharedMemorySize, K1_LDS);
  hipFuncSetAttribute((const void*)k2_attn, hipFuncAttributeMaxDynamicSharedMemorySize, K2_LDS);
  k1_proj<<<256, 512, K1_LDS, stream>>>(x, wq, wk, wv, wo, wg, wu, wd, gw, ws);
  k2_attn<<<256, 512, K2_LDS, stream>>>(ws);
  k2_down<<<224, 256, 49152, stream>>>(ws);
  k3_out<<<224, 256, 49152, stream>>>(ws, (unsigned short*)d_out);
}
```

```cpp
#include <hip/hip_runtime.h>
#include <hip/hip_bf16.h>
#include <stdint.h>

typedef __attribute__((ext_vector_type(8))) short bf16x8;
typedef __attribute__((ext_vector_type(4))) short s16x4;
typedef __attribute__((ext_vector_type(16))) float f32x16;
typedef __attribute__((ext_vector_type(4))) float f32x4;
typedef __attribute__((ext_vector_type(4))) unsigned u32x4;
typedef __attribute__((ext_vector_type(2))) unsigned u32x2;
#define LAS __attribute__((address_space(3)))
#define MFMA(a, b, c) __builtin_amdgcn_mfma_f32_32x32x16_bf16(a, b, c, 0, 0, 0)
#define SBAR() __builtin_amdgcn_sched_barrier(0)
#define PIN(x) asm volatile("" : "+v"(x))
#define DSR128(dst, addr, off) asm volatile("ds_read_b128 %0, %1 offset:%2" : "=v"(dst) : "v"(addr), "i"(off))
#define DSRTR(dst, addr, off) asm volatile("ds_read_b64_tr_b16 %0, %1 offset:%2" : "=v"(dst) : "v"(addr), "i"(off))
#define WAITVM(n) asm volatile("s_waitcnt vmcnt(%0)" ::"i"(n) : "memory")
#define BARRIER() asm volatile("s_barrier" ::: "memory")

constexpr int S_ = 2048, D_ = 448, F_ = 1792, DH = 28;
constexpr size_t WS_Q = 0;
constexpr size_t WS_KV = WS_Q + (2u << 20);
constexpr size_t WS_H = WS_KV + (256u << 10);
constexpr size_t WS_WD = WS_H + 64 * 112 * 1024;
constexpr size_t WS_WO = WS_WD + 14 * 112 * 1024;
constexpr size_t WS_O = WS_WO + 15 * 32 * 1024;
constexpr size_t WS_G = WS_O + (2u << 20);
constexpr size_t WS_CNT = WS_G + 8192;
constexpr size_t WS_DBG = 32u << 20;

#define TSDECL
#define STAMP(i)
#define TSFLUSH(k)

__device__ __forceinline__ unsigned cvtpk(float lo, float hi) {
  unsigned r; asm("v_cvt_pk_bf16_f32 %0, %1, %2" : "=v"(r) : "v"(lo), "v"(hi)); return r;
}
__device__ __forceinline__ float rbf(float x) { return __uint_as_float(cvtpk(x, 0.f) << 16); }
__device__ __forceinline__ void glds16(const void* g, LAS void* l) {
  __builtin_amdgcn_global_load_lds((const unsigned*)g, (LAS unsigned*)l, 16, 0, 0);
}
__device__ __forceinline__ bf16x8 cat8(s16x4 lo, s16x4 hi) { return (bf16x8){lo[0], lo[1], lo[2], lo[3], hi[0], hi[1], hi[2], hi[3]}; }

#define HST 0
__device__ __forceinline__ void st16(void* p, u32x4 v) {
  *(u32x4*)p = v;
}
constexpr int K1_STAGE = 49152, K1_LDS = 3 * K1_STAGE;
struct K1Frag { s16x4 al[2], ah[2]; bf16x8 b[2]; };
template <int OFFA, int OFFB>
__device__ __forceinline__ void k1_read(K1Frag& f, unsigned aaddr, unsigned baddr) {
  DSRTR(f.al[0], aaddr, OFFA); DSRTR(f.ah[0], aaddr, OFFA + 256);
  DSR128(f.b[0], baddr, OFFB);
  DSRTR(f.al[1], aaddr, OFFA + 2048); DSRTR(f.ah[1], aaddr, OFFA + 2304);
  DSR128(f.b[1], baddr, OFFB + 4096);
}
template <int N>
__device__ __forceinline__ void k1_wait(K1Frag& f) {
  asm volatile("s_waitcnt lgkmcnt(%6)" : "+v"(f.al[0]), "+v"(f.ah[0]), "+v"(f.al[1]), "+v"(f.ah[1]), "+v"(f.b[0]), "+v"(f.b[1]) : "i"(N));
}
template <bool SWAP1>
__device__ __forceinline__ void k1_mfma(K1Frag& f, f32x16 (&acc)[2][2]) {
  bf16x8 a0 = cat8(f.al[0], f.ah[0]), a1 = cat8(f.al[1], f.ah[1]);
  acc[0][0] = MFMA(a0, f.b[0], acc[0][0]);
  acc[1][0] = SWAP1 ? MFMA(f.b[0], a1, acc[1][0]) : MFMA(a1, f.b[0], acc[1][0]);
  acc[0][1] = MFMA(a0, f.b[1], acc[0][1]);
  acc[1][1] = SWAP1 ? MFMA(f.b[1], a1, acc[1][1]) : MFMA(a1, f.b[1], acc[1][1]);
}
__device__ __forceinline__ float dpp_sum64(float v) {
  v += __int_as_float(__builtin_amdgcn_update_dpp(0, __float_as_int(v), 0x111, 0xf, 0xf, true));
  v += __int_as_float(__builtin_amdgcn_update_dpp(0, __float_as_int(v), 0x112, 0xf, 0xf, true));
  v += __int_as_float(__builtin_amdgcn_update_dpp(0, __float_as_int(v), 0x114, 0xf, 0xf, true));
  v += __int_as_float(__builtin_amdgcn_update_dpp(0, __float_as_int(v), 0x118, 0xf, 0xf, true));
  v += __int_as_float(__builtin_amdgcn_update_dpp(0, __float_as_int(v), 0x142, 0xa, 0xf, true));
  v += __int_as_float(__builtin_amdgcn_update_dpp(0, __float_as_int(v), 0x143, 0xc, 0xf, true));
  return v;
}
struct K1Ctx {
  const char* wp[4]; const char* xp[2]; int wstride, wstride3; char* smem; int w;
  unsigned ab[3], bb[4];
};
__device__ __forceinline__ void k1_dma(const K1Ctx& c, int st, int buf, int i) {
  char* wb = c.smem + buf * K1_STAGE;
  if (i < 4) glds16(c.wp[i] + (size_t)st * (i == 3 ? c.wstride3 : c.wstride), (LAS void*)(wb + (4 * c.w + i) * 1024));
  else glds16(c.xp[i - 4] + st * 128, (LAS void*)(wb + 32768 + (2 * c.w + i - 4) * 1024));
}
template <bool SWAP1, typename MID>
__device__ __forceinline__ void k1_main(const K1Ctx& c, f32x16 (&acc)[2][2], MID mid) {
  K1Frag f0, f1;
  k1_read<0, 0>(f0, c.ab[0], c.bb[0]);
  #pragma unroll
  for (int st = 0; st < 7; st++) {
    const int buf = st % 3, nb = (st + 1) % 3, pb = (st + 2) % 3;
    const unsigned a_ = c.ab[buf], bo = buf * K1_STAGE, bn = nb * K1_STAGE;
    const bool dm = st >= 1 && st + 2 < 7;
    k1_read<8192, 0>(f1, a_, c.bb[1] + bo); if (dm) { k1_dma(c, st + 2, pb, 2); k1_dma(c, st + 2, pb, 3); }
    k1_wait<6>(f0); SBAR(); k1_mfma<SWAP1>(f0, acc); SBAR();
    k1_read<16384, 0>(f0, a_, c.bb[2] + bo); if (dm) { k1_dma(c, st + 2, pb, 4); k1_dma(c, st + 2, pb, 5); }
    k1_wait<6>(f1); SBAR(); k1_mfma<SWAP1>(f1, acc); SBAR();
    k1_read<24576, 0>(f1, a_, c.bb[3] + bo); k1_wait<6>(f0); SBAR(); k1_mfma<SWAP1>(f0, acc);
    if (st == 3) mid();
    SBAR();
    k1_wait<0>(f1);
    if (st < 6) {
      if (st == 0) WAITVM(16); else if (st == 1) WAITVM(6); else if (st == 3) WAITVM(8); else if (st <= 4) WAITVM(6); else WAITVM(0);
      BARRIER();
      k1_read<0, 0>(f0, c.ab[nb], c.bb[0] + bn);
      if (st + 3 < 7) { k1_dma(c, st + 3, buf, 0); k1_dma(c, st + 3, buf, 1); }
    }
    SBAR(); k1_mfma<SWAP1>(f1, acc); SBAR();
  }
}
__global__ __launch_bounds__(512, 2) void k1_proj(
    const unsigned short* __restrict__ x, const unsigned short* __restrict__ wq, const unsigned short* __restrict__ wk,
    const unsigned short* __restrict__ wv, const unsigned short* __restrict__ wo, const unsigned short* __restrict__ wg,
    const unsigned short* __restrict__ wu, const unsigned short* __restrict__ wd, const unsigned short* __restrict__ gw,
    char* __restrict__ ws) {
  extern __shared__ __attribute__((aligned(16))) char smem[];
  TSDECL; STAMP(0);
  const int tid = threadIdx.x, lane = tid & 63, w = __builtin_amdgcn_readfirstlane(tid >> 6);
  const int hh = lane >> 5, l31 = lane & 31;
  const int bid = blockIdx.x, xcd = bid & 7, idx = bid >> 3;
  const int rb = (xcd & 1) * 8 + (idx & 7), cg = (xcd >> 1) * 4 + (idx >> 3);
  const int m0 = rb * 128;
  K1Ctx c; c.smem = smem; c.w = w;
  {
    const int krow = 8 * w + ((lane >> 2) & 7), ch = lane & 3, cl = lane >> 5;
    if (cg < 14) {
      c.wp[0] = (const char*)(wg + (size_t)krow * F_ + 128 * cg + 32 * (0 + cl) + 8 * ch);
      c.wp[1] = (const char*)(wg + (size_t)krow * F_ + 128 * cg + 32 * (2 + cl) + 8 * ch);
      c.wp[2] = (const char*)(wu + (size_t)krow * F_ + 128 * cg + 32 * (0 + cl) + 8 * ch);
      c.wp[3] = (const char*)(wu + (size_t)krow * F_ + 128 * cg + 32 * (2 + cl) + 8 * ch);
      c.wstride = 64 * F_ * 2;
    } else {
      const int cb = (cg - 14) * 256;
      c.wp[0] = (const char*)(wq + (size_t)krow * D_ + cb + 32 * (0 + cl) + 8 * ch);
      c.wp[1] = (const char*)(wq + (size_t)krow * D_ + cb + 32 * (2 + cl) + 8 * ch);
      c.wp[2] = (const char*)(wq + (size_t)krow * D_ + cb + 32 * (4 + cl) + 8 * ch);
      if (cg == 14) c.wp[3] = (const char*)(wq + (size_t)krow * D_ + cb + 32 * (6 + cl) + 8 * ch);
      else c.wp[3] = (const char*)((cl ? wv : wk) + (size_t)krow * DH + (ch == 3 ? 20 : 8 * ch));
      c.wstride = 64 * D_ * 2;
    }
    c.wstride3 = (cg == 15) ? 64 * DH * 2 : c.wstride;
    #pragma unroll
    for (int i = 0; i < 2; i++) { const int m = 8 * (2 * w + i) + (lane >> 3); const int ch2 = (lane & 7) ^ ((m >> 1) & 7);
      c.xp[i] = (const char*)(x + (size_t)(m0 + m) * D_ + 8 * ch2); }
  }
  #pragma unroll
  for (int i = 0; i < 6; i++) k1_dma(c, 0, 0, i);
  SBAR(); BARRIER();
  #pragma unroll
  for (int i = 0; i < 6; i++) k1_dma(c, 1, 1, i);
  SBAR();
  const int gwave = bid * 8 + w;
  u32x4 gx, gv; unsigned pt[8];
  const bool isd = gwave < 1568;
  const int pe = isd ? gwave : (gwave < 2016 ? gwave - 1568 : 0);
  {
    const int l56 = lane < 56 ? lane : 55;
    const unsigned short* gxp = x + (size_t)gwave * D_ + l56 * 8;
    const unsigned short* gvp = gw + l56 * 8;
    asm volatile("global_load_dwordx4 %0, %1, off" : "=v"(gx) : "v"(gxp));
    asm volatile("global_load_dwordx4 %0, %1, off" : "=v"(gv) : "v"(gvp));
    const int nt = isd ? pe / 112 : pe >> 5, fs = pe % 112, head = (pe >> 1) & 15, s = pe & 1;
    const unsigned short* srcb = (isd ? wd : wo) + 32 * nt + l31;
    #pragma unroll
    for (int j = 0; j < 8; j++) {
      const int r = 8 * (j >> 2) + 4 * hh + (j & 3);
      const int r2 = 16 * s + r, d = r2 < 24 ? r2 : (r2 < 28 ? 0 : r2 - 4);
      const int row = isd ? 16 * fs + r : head * DH + d;
      const unsigned short* p = srcb + (size_t)row * D_;
      asm volatile("global_load_ushort %0, %1, off" : "=v"(pt[j]) : "v"(p));
    }
  }
  SBAR();
  #pragma unroll
  for (int i = 0; i < 6; i++) k1_dma(c, 2, 2, i);
  SBAR();

  const int wf = w & 3, wm = w >> 2;
  const unsigned lds0 = (unsigned)(uintptr_t)(LAS char*)smem;
  const unsigned abase = lds0 + 4096 * hh + 64 * ((lane & 15) >> 2) + 32 * ((lane >> 4) & 1) + 8 * (lane & 3) + 512 * wf;
  #pragma unroll
  for (int ks = 0; ks < 4; ks++) c.bb[ks] = lds0 + 32768 + 128 * (64 * wm + l31) + 16 * ((2 * ks + hh) ^ ((lane >> 1) & 7));
  c.ab[0] = abase; c.ab[1] = abase + K1_STAGE; c.ab[2] = abase + 2 * K1_STAGE;
  unsigned qoff[2][4];
  #pragma unroll
  for (int a = 0; a < 2; a++)
    #pragma unroll
    for (int g = 0; g < 4; g++) {
      const unsigned G = 8 * ((cg - 14) * 8 + wf + 4 * a) + 2 * g + hh, head = (G * 9363u) >> 16, u = G - 7 * head, pos = (u == 6) ? 7 : u;
      qoff[a][g] = head * 2048 + (pos >> 2) * 1024 + ((pos & 1) * 32 + l31) * 16 + ((pos >> 1) & 1) * 8;
      PIN(qoff[a][g]);
    }
  f32x16 acc[2][2];
  #pragma unroll
  for (int a = 0; a < 2; a++) for (int b = 0; b < 2; b++) for (int r = 0; r < 16; r++) acc[a][b][r] = 0.f;

  auto mid = [&]() {
    asm volatile("" : "+v"(gx), "+v"(gv), "+v"(pt[0]), "+v"(pt[1]), "+v"(pt[2]), "+v"(pt[3]), "+v"(pt[4]), "+v"(pt[5]), "+v"(pt[6]), "+v"(pt[7]));
    u32x4 o = {(pt[0] & 0xffffu) | (pt[1] << 16), (pt[2] & 0xffffu) | (pt[3] << 16), (pt[4] & 0xffffu) | (pt[5] << 16), (pt[6] & 0xffffu) | (pt[7] << 16)};
    if (!isd && (pe & 1) && hh == 0) { o[2] = 0u; o[3] = 0u; }
    char* dst = isd ? ws + WS_WD + (size_t)gwave * 1024 : ws + WS_WO + (size_t)(gwave - 1568) * 1024;
    asm volatile("global_store_dwordx4 %0, %1, off\n s_nop 1" ::"v"(dst + lane * 16), "v"(o) : "memory");
    float sum = 0.f;
    #pragma unroll
    for (int i = 0; i < 4; i++) {
      sum += __uint_as_float(gx[i] << 16) * __uint_as_float(gv[i] << 16);
      sum += __uint_as_float(gx[i] & 0xffff0000u) * __uint_as_float(gv[i] & 0xffff0000u);
    }
    if (lane >= 56) sum = 0.f;
    sum = dpp_sum64(sum);
    const float gte = 1.f / (1.f + __expf(-sum));
    float* gp = (float*)(ws + WS_G) + gwave;
    if (lane == 63) asm volatile("global_store_dword %0, %1, off" ::"v"(gp), "v"(gte) : "memory");
  };

  WAITVM(22); BARRIER(); STAMP(1);
  const bool vwave = (cg == 15) && (wf == 3);
  if (vwave) k1_main<true>(c, acc, mid); else k1_main<false>(c, acc, mid);
  STAMP(2);

  if (cg < 14) {
    #pragma unroll
    for (int b = 0; b < 2; b++) {
      const int tt = 4 * rb + 2 * wm + b, ft = 4 * cg + wf;
      #pragma unroll
      for (int s = 0; s < 2; s++) {
        u32x4 o;
        #pragma unroll
        for (int j = 0; j < 4; j++) {
          const int r = 8 * s + 2 * j;
          o[j] = cvtpk(acc[0][b][r] * acc[1][b][r], acc[0][b][r + 1] * acc[1][b][r + 1]);
        }
        st16(ws + WS_H + ((size_t)(tt * 112 + 2 * ft + s) * 64 + lane) * 16, o);
      }
    }
  } else {
    const float qs = 0.18898223650461361f * 1.4426950408889634f;
    #pragma unroll
    for (int a = 0; a < 2; a++) {
      const int cglob = (cg - 14) * 8 + wf + 4 * a;
      #pragma unroll
      for (int b = 0; b < 2; b++) {
        const int tt = 4 * rb + 2 * wm + b;
        if (cglob < 14) {
          char* base = ws + WS_Q + (size_t)tt * 32768;
          #pragma unroll
          for (int g = 0; g < 4; g++) {
            u32x2 v = {cvtpk(acc[a][b][4 * g] * qs, acc[a][b][4 * g + 1] * qs), cvtpk(acc[a][b][4 * g + 2] * qs, acc[a][b][4 * g + 3] * qs)};
            *(u32x2*)(base + qoff[a][g]) = v;
          }
        } else {
          #pragma unroll
          for (int s = 0; s < 2; s++) {
            u32x4 o;
            #pragma unroll
            for (int j = 0; j < 4; j++) o[j] = cvtpk(acc[a][b][8 * s + 2 * j], acc[a][b][8 * s + 2 * j + 1]);
            if (cglob == 14) { if (s == 1 && hh == 0) { o[2] = 0u; o[3] = 0u; } }
            else if (l31 >= 24 && l31 < 28) { const unsigned f = (l31 == 24) ? 0x3f803f80u : 0u; o[0] = f; o[1] = f; o[2] = f; o[3] = f; }
            st16(ws + WS_KV + (size_t)tt * 4096 + (cglob - 14) * 2048 + s * 1024 + lane * 16, o);
          }
        }
      }
    }
  }
  if (bid < 32 && tid == 0) *(unsigned*)(ws + WS_CNT + bid * 256) = 0u;
  STAMP(3); TSFLUSH(0);
}

typedef __amdgpu_buffer_rsrc_t rsrc_t;
#define BLD(r, vo, so, imm) __builtin_amdgcn_raw_buffer_load_b128(r, (vo) + (imm), so, 0)
#define MF_S0(n, k, q) asm volatile("v_mfma_f32_32x32x16_bf16 %0, %1, %2, 0" : "=&v"(n) : "v"(k), "a"(q))
#define MF_S1(n, k, q) asm volatile("v_mfma_f32_32x32x16_bf16 %0, %1, %2, %0" : "+v"(n) : "v"(k), "a"(q))
#define MF_O(o, v, p) asm volatile("v_mfma_f32_32x32x16_bf16 %0, %1, %2, %0" : "+a"(o) : "v"(v), "v"(p))
#define MF_DV(d, a, b) asm volatile("v_mfma_f32_32x32x16_bf16 %0, %1, %2, %0" : "+a"(d) : "v"(a), "v"(b))
#define MF_DA(d, a, b) asm volatile("v_mfma_f32_32x32x16_bf16 %0, %1, %2, %0" : "+a"(d) : "a"(a), "a"(b))
#define EXP2(x) __builtin_amdgcn_exp2f(x)
constexpr int K2_LDS = 98304;
struct KVF { u32x4 k0, k1, v0, v1; };
struct DKF { u32x4 a0, a1, b0, b1; };
__device__ __forceinline__ unsigned ptrunc(float lo, float hi) { return __builtin_amdgcn_perm(__float_as_uint(hi), __float_as_uint(lo), 0x07060302u); }
struct AttnState {
  f32x16 S[2], O[4], D[2][2];
  u32x4 q[4][2], P1; float e14, e15;
  KVF kv[4]; DKF dk[8];
  rsrc_t rW, rLo, rHi; unsigned vKV, vA0, vA1, vB0, vB1;
};
template <int SLOT> __device__ __forceinline__ void mf_d(f32x16& d, const u32x4& a, const u32x4& b) { if (SLOT < 4) MF_DV(d, a, b); else MF_DA(d, a, b); }
template <int SLOT, int PC> __device__ __forceinline__ void dk_ld(AttnState& t, rsrc_t r, unsigned so) {
  constexpr int imm = (SLOT & 3) * 1024;
  if (PC == 0) t.dk[SLOT].b1 = BLD(r, t.vB1, so, imm);
  if (PC == 1) t.dk[SLOT].a1 = BLD(r, t.vA1, so, imm);
  if (PC == 2) t.dk[SLOT].b0 = BLD(r, t.vB0, so, imm);
  if (PC == 3) t.dk[SLOT].a0 = BLD(r, t.vA0, so, imm);
}
template <int SLOT, int PC> __device__ __forceinline__ void kv_ld(AttnState& t, unsigned so) {
  if (PC == 0) t.kv[SLOT].v1 = BLD(t.rW, t.vKV, so, 3072);
  if (PC == 1) t.kv[SLOT].v0 = BLD(t.rW, t.vKV, so, 2048);
  if (PC == 2) t.kv[SLOT].k1 = BLD(t.rW, t.vKV, so, 1024);
  if (PC == 3) t.kv[SLOT].k0 = BLD(t.rW, t.vKV, so, 0);
}
template <int U, int J, bool DOWN, bool NEXT, bool KVL, bool DLA, bool DLB>
__device__ __forceinline__ void attn_step(AttnState& t, unsigned sokv, unsigned sodA, unsigned sodB) {
  constexpr int PU = (U + 3) & 3, NU = (U + 1) & 3;
  constexpr int DS = 2 * U + (J >> 1);
  constexpr int RS = (J < 2) ? ((2 * U + 7) & 7) : 2 * U;
  constexpr bool DL = (J < 2) ? DLA : DLB;
  f32x16& Sc = t.S[J & 1]; f32x16& Sn = t.S[(J + 1) & 1];
  const KVF& kc = t.kv[U]; const KVF& kk = (J == 3) ? t.kv[NU] : t.kv[U];
  const unsigned sod = (J < 2) ? sodA : sodB;
  const rsrc_t rd = (RS >= 4 && RS < 7) ? t.rHi : t.rLo;
  float e0, e1, e2, e3, e4, e5, e6, e7, e8, e9, e10, e11, e12, e13; u32x4 P0;
  if (NEXT) MF_S0(Sn, kk.k0, t.q[(J + 1) & 3][0]);
  SBAR();
  t.P1[3] = ptrunc(t.e14, t.e15); SBAR();
  e0 = EXP2(Sc[0]); e1 = EXP2(Sc[1]); e2 = EXP2(Sc[2]);
  SBAR();
  if (J == 0) MF_O(t.O[3], t.kv[PU].v1, t.P1); else MF_O(t.O[J - 1], kc.v1, t.P1);
  SBAR();
  e3 = EXP2(Sc[3]); e4 = EXP2(Sc[4]); e5 = EXP2(Sc[5]); P0[0] = ptrunc(e0, e1);
  SBAR();
  if (NEXT) MF_S1(Sn, kk.k1, t.q[(J + 1) & 3][1]);
  SBAR();
  e6 = EXP2(Sc[6]); e7 = EXP2(Sc[7]); P0[1] = ptrunc(e2, e3); P0[2] = ptrunc(e4, e5);
  if (KVL && J < 2) kv_ld<PU, 2 * J>(t, sokv);
  if (DL && J >= 2) dk_ld<RS, 2 * (J & 1)>(t, rd, sod);
  SBAR();
  if (DOWN) { if (J & 1) mf_d<DS>(t.D[0][1], t.dk[DS].a0, t.dk[DS].b1); else mf_d<DS>(t.D[0][0], t.dk[DS].a0, t.dk[DS].b0); }
  SBAR();
  P0[3] = ptrunc(e6, e7); SBAR();
  e8 = EXP2(Sc[8]); e9 = EXP2(Sc[9]); e10 = EXP2(Sc[10]);
  if (DL && J < 2) dk_ld<RS, 2 * (J & 1)>(t, rd, sod);
  SBAR();
  MF_O(t.O[J], kc.v0, P0);
  SBAR();
  e11 = EXP2(Sc[11]); e12 = EXP2(Sc[12]); e13 = EXP2(Sc[13]); t.P1[0] = ptrunc(e8, e9);
  if (DL && J < 2) dk_ld<RS, 2 * (J & 1) + 1>(t, rd, sod);
  SBAR();
  if (DOWN) { if (J & 1) mf_d<DS>(t.D[1][1], t.dk[DS].a1, t.dk[DS].b1); else mf_d<DS>(t.D[1][0], t.dk[DS].a1, t.dk[DS].b0); }
  SBAR();
  t.e14 = EXP2(Sc[14]); t.e15 = EXP2(Sc[15]); t.P1[1] = ptrunc(e10, e11); t.P1[2] = ptrunc(e12, e13);
  if (KVL && J < 2) kv_ld<PU, 2 * J + 1>(t, sokv);
  if (DL && J >= 2) dk_ld<RS, 2 * (J & 1) + 1>(t, rd, sod);
  SBAR();
}
template <int U, bool DOWN, bool LAST, bool KVL, bool DLA, bool DLB>
__device__ __forceinline__ void attn_tile(AttnState& t, unsigned sokv, unsigned sodA, unsigned sodB) {
  attn_step<U, 0, DOWN, true, KVL, DLA, DLB>(t, sokv, sodA, sodB);
  attn_step<U, 1, DOWN, true, KVL, DLA, DLB>(t, sokv, sodA, sodB);
  attn_step<U, 2, DOWN, true, KVL, DLA, DLB>(t, sokv, sodA, sodB);
  attn_step<U, 3, DOWN, !LAST, KVL, DLA, DLB>(t, sokv, sodA, sodB);
}
__device__ __forceinline__ void lds_put(char* p, int lane, const f32x16& v) {
  #pragma unroll
  for (int g = 0; g < 4; g++) *(f32x4*)(p + g * 1024 + lane * 16) = (f32x4){v[4 * g], v[4 * g + 1], v[4 * g + 2], v[4 * g + 3]};
}
__device__ __forceinline__ void lds_add(const char* p, int lane, f32x16& o) {
  #pragma unroll
  for (int g = 0; g < 4; g++) { f32x4 v = *(const f32x4*)(p + g * 1024 + lane * 16);
    o[4 * g] += v[0]; o[4 * g + 1] += v[1]; o[4 * g + 2] += v[2]; o[4 * g + 3] += v[3]; }
}
__device__ __forceinline__ void red_put(char* red, int kq, int lane, const f32x16& t0, const f32x16& t1, const f32x16& t2, const f32x16& t3) {
  if (kq != 0) lds_put(red + (0 * 3 + kq - 1) * 4096, lane, t0);
  if (kq != 1) lds_put(red + (1 * 3 + (kq < 1 ? kq : kq - 1)) * 4096, lane, t1);
  if (kq != 2) lds_put(red + (2 * 3 + (kq < 2 ? kq : kq - 1)) * 4096, lane, t2);
  if (kq != 3) lds_put(red + (3 * 3 + kq) * 4096, lane, t3);
}
__device__ __forceinline__ f32x16 red_get(const char* red, int kq, int lane, const f32x16& t0, const f32x16& t1, const f32x16& t2, const f32x16& t3) {
  f32x16 o;
  if (kq == 0) o = t0; else if (kq == 1) o = t1; else if (kq == 2) o = t2; else o = t3;
  #pragma unroll
  for (int sl = 0; sl < 3; sl++) lds_add(red + (kq * 3 + sl) * 4096, lane, o);
  return o;
}
__global__ __launch_bounds__(256, 1) void k2_fused(char* __restrict__ ws, unsigned short* __restrict__ out) {
  extern __shared__ __attribute__((aligned(16))) char smem[];
  TSDECL; STAMP(0);
  const int tid = threadIdx.x, lane = tid & 63, kq = __builtin_amdgcn_readfirstlane(tid >> 6);
  const int hh = lane >> 5, l31 = lane & 31;
  const int bid = blockIdx.x, xcd = bid & 7, idx = bid >> 3;
  const int ttile = xcd * 8 + (idx >> 2), qt0 = ttile * 16 + 4 * (idx & 3);
  const bool job = idx < 28;
  const int tp = xcd * 4 + (idx & 3), np = job ? (idx >> 2) : 0;
  constexpr int WSN = 1 << 25;
  const rsrc_t rW = __builtin_amdgcn_make_buffer_rsrc(ws, 0, WSN, 0x00020000);
  const rsrc_t rJ = __builtin_amdgcn_make_buffer_rsrc(ws, 0, job ? WSN : 0, 0x00020000);
  AttnState t;
  t.rW = rW; t.rLo = rJ; t.rHi = rJ;
  t.vKV = (unsigned)WS_KV + kq * 65536 + lane * 16;
  t.vA0 = (unsigned)WS_WD + ((2 * np) * 112 + 28 * kq) * 1024 + lane * 16; t.vA1 = t.vA0 + 112 * 1024;
  t.vB0 = (unsigned)WS_H + ((2 * tp) * 112 + 28 * kq) * 1024 + lane * 16; t.vB1 = t.vB0 + 112 * 1024;
  {
    const unsigned vq = (unsigned)WS_Q + qt0 * 2048 + lane * 16;
    kv_ld<0, 3>(t, 0); kv_ld<0, 2>(t, 0);
    u32x4 q1[4];
    #pragma unroll
    for (int j = 0; j < 4; j++) { t.q[j][0] = BLD(rW, vq, j * 2048, 0); q1[j] = BLD(rW, vq, j * 2048, 1024); }
    kv_ld<0, 1>(t, 0); kv_ld<0, 0>(t, 0);
    SBAR();
#define DKG(S_, SO) dk_ld<S_, 0>(t, rJ, SO); SBAR(); dk_ld<S_, 1>(t, rJ, SO); SBAR(); dk_ld<S_, 2>(t, rJ, SO); SBAR(); dk_ld<S_, 3>(t, rJ, SO); SBAR()
#define KVG(S_, SO) kv_ld<S_, 0>(t, SO); SBAR(); kv_ld<S_, 1>(t, SO); SBAR(); kv_ld<S_, 2>(t, SO); SBAR(); kv_ld<S_, 3>(t, SO); SBAR()
    DKG(0, 0); KVG(1, 4096); DKG(1, 0); DKG(2, 0); KVG(2, 8192); DKG(3, 0); DKG(4, 4096); DKG(5, 4096); DKG(6, 4096);
    #pragma unroll
    for (int j = 0; j < 4; j++)
      #pragma unroll
      for (int r = 0; r < 16; r++) { t.O[j][r] = 0.f; t.D[j >> 1][j & 1][r] = 0.f; }
    asm volatile("" : "+a"(t.O[0]), "+a"(t.O[1]), "+a"(t.O[2]), "+a"(t.O[3]), "+a"(t.D[0][0]), "+a"(t.D[0][1]), "+a"(t.D[1][0]), "+a"(t.D[1][1]));
    SBAR();
    #pragma unroll
    for (int j = 0; j < 4; j++) {
      if (hh == 0) { q1[j][2] = 0u; q1[j][3] = 0u; }
      t.q[j][1] = q1[j];
    }
    t.P1 = (u32x4){0u, 0u, 0u, 0u}; t.e14 = 0.f; t.e15 = 0.f; t.kv[3].v1 = (u32x4){0u, 0u, 0u, 0u};
    asm volatile("s_nop 1" : "+a"(t.q[0][0]), "+a"(t.q[0][1]), "+a"(t.q[1][1]), "+a"(t.q[2][1]), "+a"(t.q[3][1]), "+a"(t.O[0]), "+a"(t.O[1]), "+a"(t.O[2]), "+a"(t.O[3]),
                 "+a"(t.D[0][0]), "+a"(t.D[0][1]), "+a"(t.D[1][0]), "+a"(t.D[1][1]), "+v"(t.kv[3].v1));
    MF_S0(t.S[0], t.kv[0].k0, t.q[0][0]); MF_S1(t.S[0], t.kv[0].k1, t.q[0][1]);
    asm volatile("s_nop 15" : "+v"(t.S[0]));
    SBAR();
  }
  STAMP(1);
  #pragma unroll 1
  for (int i = 0; i < 3; i++) {
    const unsigned sk = i * 16384, sd = i * 8192;
    t.rHi = __builtin_amdgcn_make_buffer_rsrc(ws, 0, (job && i < 2) ? WSN : 0, 0x00020000);
    attn_tile<0, true, false, true, true, true>(t, sk + 3 * 4096, sd + 4096, sd + 8192);
    attn_tile<1, true, false, true, true, true>(t, sk + 4 * 4096, sd + 8192, sd + 8192);
    attn_tile<2, true, false, true, true, true>(t, sk + 5 * 4096, sd + 8192, sd + 8192 + 4096);
    attn_tile<3, true, false, true, true, true>(t, sk + 6 * 4096, sd + 8192 + 4096, sd + 8192 + 4096);
  }
  attn_tile<0, true, false, true, false, false>(t, 15 * 4096, 0, 0);
  attn_tile<1, true, false, false, false, false>(t, 0, 0, 0);
  attn_tile<2, false, false, false, false, false>(t, 0, 0, 0);
  attn_tile<3, false, true, false, false, false>(t, 0, 0, 0);
  t.P1[3] = ptrunc(t.e14, t.e15);
  asm volatile("s_nop 1" : "+v"(t.P1));
  MF_O(t.O[3], t.kv[3].v1, t.P1);
  STAMP(2);
  u32x4 wo[2][8];
  {
    const unsigned vw = (unsigned)WS_WO + ((2 * np) * 32 + 8 * kq) * 1024 + lane * 16;
    #pragma unroll
    for (int k = 0; k < 8; k++) { wo[0][k] = BLD(rJ, vw, (k >> 2) * 4096, (k & 3) * 1024); wo[1][k] = BLD(rJ, vw, 32768 + (k >> 2) * 4096, (k & 3) * 1024); }
  }
  const int nt = 2 * np + (kq >> 1), tt = 2 * tp + (kq & 1);
  const float gt = ((const float*)(ws + WS_G))[tt * 32 + l31];
  asm volatile("s_nop 15" : "+a"(t.O[0]), "+a"(t.O[1]), "+a"(t.O[2]), "+a"(t.O[3]), "+a"(t.D[0][0]), "+a"(t.D[0][1]), "+a"(t.D[1][0]), "+a"(t.D[1][1]));
  red_put(smem, kq, lane, t.O[0], t.O[1], t.O[2], t.O[3]);
  red_put(smem + 49152, kq, lane, t.D[0][0], t.D[0][1], t.D[1][0], t.D[1][1]);
  __syncthreads();
  f32x16 o = red_get(smem, kq, lane, t.O[0], t.O[1], t.O[2], t.O[3]);
  const int qt = qt0 + kq;
  {
    float l = __shfl(o[12], lane & 31);
    bool bad = !(l >= 8.673617379884035e-19f && l <= 1.152921504606847e18f);
    #pragma unroll
    for (int r = 0; r < 16; r++) bad |= !(__builtin_fabsf(o[r]) <= 3.0e38f);
    if (__builtin_expect(__any(bad), 0)) {
      const char* kva = ws + WS_KV + lane * 16;
      const bf16x8 a0 = *(const bf16x8*)(ws + WS_Q + (size_t)qt * 2048 + lane * 16);
      u32x4 t1 = *(const u32x4*)(ws + WS_Q + (size_t)qt * 2048 + 1024 + lane * 16); if (hh == 0) { t1[2] = 0u; t1[3] = 0u; }
      const bf16x8 a1 = __builtin_bit_cast(bf16x8, t1);
      const f32x16 Z = {0, 0, 0, 0, 0, 0, 0, 0, 0, 0, 0, 0, 0, 0, 0, 0};
      float m = -__builtin_inff();
      #pragma unroll 1
      for (int k = 0; k < 64; k++) {
        const char* p = kva + (size_t)k * 4096;
        f32x16 Sx = MFMA(*(const bf16x8*)p, a0, Z); Sx = MFMA(*(const bf16x8*)(p + 1024), a1, Sx);
        #pragma unroll
        for (int r = 0; r < 16; r++) m = __builtin_fmaxf(m, Sx[r]);
      }
      m = __builtin_fmaxf(m, __shfl_xor(m, 32));
      o = Z;
      #pragma unroll 1
      for (int k = 0; k < 64; k++) {
        const char* p = kva + (size_t)k * 4096;
        f32x16 Sx = MFMA(*(const bf16x8*)p, a0, Z); Sx = MFMA(*(const bf16x8*)(p + 1024), a1, Sx);
        u32x4 p0, p1;
        #pragma unroll
        for (int j = 0; j < 4; j++) {
          p0[j] = cvtpk(EXP2(Sx[2 * j] - m), EXP2(Sx[2 * j + 1] - m));
          p1[j] = cvtpk(EXP2(Sx[8 + 2 * j] - m), EXP2(Sx[8 + 2 * j + 1] - m));
        }
        o = MFMA(*(const bf16x8*)(p + 2048), __builtin_bit_cast(bf16x8, p0), o);
        o = MFMA(*(const bf16x8*)(p + 3072), __builtin_bit_cast(bf16x8, p1), o);
      }
      l = __shfl(o[12], lane & 31);
    }
    const float inv = 1.f / l;
    u32x4 o0, o1;
    #pragma unroll
    for (int j = 0; j < 4; j++) { o0[j] = cvtpk(o[2 * j] * inv, o[2 * j + 1] * inv); o1[j] = cvtpk(o[8 + 2 * j] * inv, o[8 + 2 * j + 1] * inv); }
    if (hh == 0) { o1[2] = 0u; o1[3] = 0u; }
    const unsigned vo = (unsigned)WS_O + qt * 2048 + lane * 16;
    __builtin_amdgcn_raw_buffer_store_b128(o0, rW, vo, 0, 16);
    __builtin_amdgcn_raw_buffer_store_b128(o1, rW, vo + 1024, 0, 16);
  }
  const f32x16 bsum = red_get(smem + 49152, kq, lane, t.D[0][0], t.D[0][1], t.D[1][0], t.D[1][1]);
  asm volatile("s_waitcnt vmcnt(0)" ::: "memory");
  __syncthreads();
  unsigned* cnt = (unsigned*)(ws + WS_CNT);
  if (tid == 0) __hip_atomic_fetch_add(cnt + (ttile >> 1) * 64, 1u, __ATOMIC_RELAXED, __HIP_MEMORY_SCOPE_AGENT);
  STAMP(3);
  if (!job) { TSFLUSH(1); return; }
  if (kq == 0) {
    for (unsigned spins = 0; spins < (1u << 22); spins++) {
      if ((unsigned)__builtin_amdgcn_readfirstlane(__hip_atomic_load(cnt + tp * 64, __ATOMIC_RELAXED, __HIP_MEMORY_SCOPE_AGENT)) >= 8u) break;
      __builtin_amdgcn_s_sleep(1);
    }
    __builtin_amdgcn_fence(__ATOMIC_ACQUIRE, "agent");
    asm volatile("s_waitcnt vmcnt(0)" ::: "memory");
  }
  __syncthreads();
  f32x16 acc[2][2];
  #pragma unroll
  for (int a = 0; a < 2; a++) for (int b = 0; b < 2; b++) for (int r = 0; r < 16; r++) acc[a][b][r] = 0.f;
  {
    const unsigned vo = (unsigned)WS_O + ((2 * tp) * 32 + 8 * kq) * 1024 + lane * 16;
    u32x4 ob[2][8];
    #pragma unroll
    for (int k = 0; k < 8; k++) { ob[0][k] = BLD(rW, vo, (k >> 2) * 4096, (k & 3) * 1024); ob[1][k] = BLD(rW, vo, 32768 + (k >> 2) * 4096, (k & 3) * 1024); }
    SBAR();
    #pragma unroll
    for (int k = 0; k < 8; k++)
      #pragma unroll
      for (int a = 0; a < 2; a++)
        #pragma unroll
        for (int b = 0; b < 2; b++) acc[a][b] = MFMA(__builtin_bit_cast(bf16x8, wo[a][k]), __builtin_bit_cast(bf16x8, ob[b][k]), acc[a][b]);
  }
  red_put(smem, kq, lane, acc[0][0], acc[0][1], acc[1][0], acc[1][1]);
  __syncthreads();
  const f32x16 av = red_get(smem, kq, lane, acc[0][0], acc[0][1], acc[1][0], acc[1][1]);
  const float og = 1.f - gt;
  unsigned short* orow = out + (size_t)(tt * 32 + l31) * D_ + nt * 32 + 4 * hh;
  #pragma unroll
  for (int g = 0; g < 4; g++) {
    float v[4];
    #pragma unroll
    for (int e = 0; e < 4; e++) v[e] = gt * av[4 * g + e] + og * bsum[4 * g + e];
    *(u32x2*)(orow + 8 * g) = (u32x2){cvtpk(v[0], v[1]), cvtpk(v[2], v[3])};
  }
  TSFLUSH(1);
}

extern "C" void kernel_launch(void* const* d_in, const int* in_sizes, int n_in, void* d_out, int out_size, void* d_ws, size_t ws_size, hipStream_t stream) {
  const unsigned short* x = (const unsigned short*)d_in[0];
  const unsigned short* wq = (const unsigned short*)d_in[1];
  const unsigned short* wk = (const unsigned short*)d_in[2];
  const unsigned short* wv = (const unsigned short*)d_in[3];
  const unsigned short* wo = (const unsigned short*)d_in[4];
  const unsigned short* wg = (const unsigned short*)d_in[6];
  const unsigned short* wu = (const unsigned short*)d_in[7];
  const unsigned short* wd = (const unsigned short*)d_in[8];
  const unsigned short* gw = (const unsigned short*)d_in[9];
  char* ws = (char*)d_ws;
  (void)hipFuncSetAttribute((const void*)k1_proj, hipFuncAttributeMaxDynamicSharedMemorySize, K1_LDS);
  (void)hipFuncSetAttribute((const void*)k2_fused, hipFuncAttributeMaxDynamicSharedMemorySize, K2_LDS);
  k1_proj<<<256, 512, K1_LDS, stream>>>(x, wq, wk, wv, wo, wg, wu, wd, gw, ws);
  k2_fused<<<256, 256, K2_LDS, stream>>>(ws, (unsigned short*)d_out);
}
```

```cpp
#include <hip/hip_runtime.h>
#include <hip/hip_bf16.h>
#include <stdint.h>

typedef __attribute__((ext_vector_type(8))) short bf16x8;
typedef __attribute__((ext_vector_type(4))) short s16x4;
typedef __attribute__((ext_vector_type(16))) float f32x16;
typedef __attribute__((ext_vector_type(4))) float f32x4;
typedef __attribute__((ext_vector_type(4))) unsigned u32x4;
typedef __attribute__((ext_vector_type(2))) unsigned u32x2;
#define LAS __attribute__((address_space(3)))
#define MFMA(a, b, c) __builtin_amdgcn_mfma_f32_32x32x16_bf16(a, b, c, 0, 0, 0)
#define SBAR() __builtin_amdgcn_sched_barrier(0)
#define PIN(x) asm volatile("" : "+v"(x))
#define DSR128(dst, addr, off) asm volatile("ds_read_b128 %0, %1 offset:%2" : "=v"(dst) : "v"(addr), "i"(off))
#define DSRTR(dst, addr, off) asm volatile("ds_read_b64_tr_b16 %0, %1 offset:%2" : "=v"(dst) : "v"(addr), "i"(off))
#define WAITVM(n) asm volatile("s_waitcnt vmcnt(%0)" ::"i"(n) : "memory")
#define BARRIER() asm volatile("s_barrier" ::: "memory")

constexpr int S_ = 2048, D_ = 448, F_ = 1792, DH = 28;
constexpr size_t WS_Q = 0;
constexpr size_t WS_KV = WS_Q + (2u << 20);
constexpr size_t WS_H = WS_KV + (256u << 10);
constexpr size_t WS_WD = WS_H + 64 * 112 * 1024;
constexpr size_t WS_WO = WS_WD + 14 * 112 * 1024;
constexpr size_t WS_O = WS_WO + 15 * 32 * 1024;
constexpr size_t WS_G = WS_O + (2u << 20);
constexpr size_t WS_CNT = WS_G + 8192;
constexpr size_t WS_DBG = 32u << 20;

#define TSDECL
#define STAMP(i)
#define TSFLUSH(k)

__device__ __forceinline__ unsigned cvtpk(float lo, float hi) {
  unsigned r; asm("v_cvt_pk_bf16_f32 %0, %1, %2" : "=v"(r) : "v"(lo), "v"(hi)); return r;
}
__device__ __forceinline__ float rbf(float x) { return __uint_as_float(cvtpk(x, 0.f) << 16); }
__device__ __forceinline__ void glds16(const void* g, LAS void* l) {
  __builtin_amdgcn_global_load_lds((const unsigned*)g, (LAS unsigned*)l, 16, 0, 0);
}
__device__ __forceinline__ bf16x8 cat8(s16x4 lo, s16x4 hi) { return (bf16x8){lo[0], lo[1], lo[2], lo[3], hi[0], hi[1], hi[2], hi[3]}; }

#define HST 3
__device__ __forceinline__ void st16(void* p, u32x4 v) {
  asm volatile("global_store_dwordx4 %0, %1, off sc1\n s_nop 1" ::"v"(p), "v"(v) : "memory");
}
constexpr int K1_STAGE = 49152, K1_SYNC = 3 * K1_STAGE, K1_LDS = K1_SYNC + 64;
struct K1Frag { s16x4 al[2], ah[2]; bf16x8 b[2]; };
template <int OFFA, int OFFB>
__device__ __forceinline__ void k1_read(K1Frag& f, unsigned aaddr, unsigned baddr) {
  DSRTR(f.al[0], aaddr, OFFA); DSRTR(f.ah[0], aaddr, OFFA + 256);
  DSR128(f.b[0], baddr, OFFB);
  DSRTR(f.al[1], aaddr, OFFA + 2048); DSRTR(f.ah[1], aaddr, OFFA + 2304);
  DSR128(f.b[1], baddr, OFFB + 4096);
}
template <int N>
__device__ __forceinline__ void k1_wait(K1Frag& f) {
  asm volatile("s_waitcnt lgkmcnt(%6)" : "+v"(f.al[0]), "+v"(f.ah[0]), "+v"(f.al[1]), "+v"(f.ah[1]), "+v"(f.b[0]), "+v"(f.b[1]) : "i"(N));
}
template <bool SWAP1>
__device__ __forceinline__ void k1_mfma(K1Frag& f, f32x16 (&acc)[2][2]) {
  bf16x8 a0 = cat8(f.al[0], f.ah[0]), a1 = cat8(f.al[1], f.ah[1]);
  acc[0][0] = MFMA(a0, f.b[0], acc[0][0]);
  acc[1][0] = SWAP1 ? MFMA(f.b[0], a1, acc[1][0]) : MFMA(a1, f.b[0], acc[1][0]);
  acc[0][1] = MFMA(a0, f.b[1], acc[0][1]);
  acc[1][1] = SWAP1 ? MFMA(f.b[1], a1, acc[1][1]) : MFMA(a1, f.b[1], acc[1][1]);
}
__device__ __forceinline__ float dpp_sum64(float v) {
  v += __int_as_float(__builtin_amdgcn_update_dpp(0, __float_as_int(v), 0x111, 0xf, 0xf, true));
  v += __int_as_float(__builtin_amdgcn_update_dpp(0, __float_as_int(v), 0x112, 0xf, 0xf, true));
  v += __int_as_float(__builtin_amdgcn_update_dpp(0, __float_as_int(v), 0x114, 0xf, 0xf, true));
  v += __int_as_float(__builtin_amdgcn_update_dpp(0, __float_as_int(v), 0x118, 0xf, 0xf, true));
  v += __int_as_float(__builtin_amdgcn_update_dpp(0, __float_as_int(v), 0x142, 0xa, 0xf, true));
  v += __int_as_float(__builtin_amdgcn_update_dpp(0, __float_as_int(v), 0x143, 0xc, 0xf, true));
  return v;
}
struct K1Ctx {
  const char* wp[4]; const char* xp[2]; int wstride, wstride3; char* smem; int w;
  unsigned ab[3], bb[4];
};
__device__ __forceinline__ void k1_dma(const K1Ctx& c, int st, int buf, int i) {
  char* wb = c.smem + buf * K1_STAGE;
  if (i < 4) glds16(c.wp[i] + (size_t)st * (i == 3 ? c.wstride3 : c.wstride), (LAS void*)(wb + (4 * c.w + i) * 1024));
  else glds16(c.xp[i - 4] + st * 128, (LAS void*)(wb + 32768 + (2 * c.w + i - 4) * 1024));
}
template <bool SWAP1, typename MID, typename SIDE>
__device__ __forceinline__ void k1_main(const K1Ctx& c, f32x16 (&acc)[2][2], MID mid, SIDE side) {
  K1Frag f0, f1;
  k1_read<0, 0>(f0, c.ab[0], c.bb[0]);
  #pragma unroll
  for (int st = 0; st < 7; st++) {
    const int buf = st % 3, nb = (st + 1) % 3, pb = (st + 2) % 3;
    const unsigned a_ = c.ab[buf], bo = buf * K1_STAGE, bn = nb * K1_STAGE;
    const bool dm = st >= 1 && st + 2 < 7;
    k1_read<8192, 0>(f1, a_, c.bb[1] + bo); if (dm) { k1_dma(c, st + 2, pb, 2); k1_dma(c, st + 2, pb, 3); }
    if (st == 0) { k1_dma(c, 2, 2, 0); k1_dma(c, 2, 2, 1); k1_dma(c, 2, 2, 2); }
    k1_wait<6>(f0); SBAR(); k1_mfma<SWAP1>(f0, acc); SBAR();
    k1_read<16384, 0>(f0, a_, c.bb[2] + bo); if (dm) { k1_dma(c, st + 2, pb, 4); k1_dma(c, st + 2, pb, 5); }
    if (st == 0) { k1_dma(c, 2, 2, 3); k1_dma(c, 2, 2, 4); k1_dma(c, 2, 2, 5); }
    k1_wait<6>(f1); SBAR(); k1_mfma<SWAP1>(f1, acc); SBAR();
    k1_read<24576, 0>(f1, a_, c.bb[3] + bo);
    if (st == 0) side();
    k1_wait<6>(f0); SBAR(); k1_mfma<SWAP1>(f0, acc);
    if (st == 3) mid();
    SBAR();
    k1_wait<0>(f1);
    if (st < 6) {

      if (st <= 1) WAITVM(16); else if (st == 3) WAITVM(8); else if (st <= 4) WAITVM(6); else WAITVM(0);
      BARRIER();
      k1_read<0, 0>(f0, c.ab[nb], c.bb[0] + bn);
      if (st + 3 < 7) { k1_dma(c, st + 3, buf, 0); k1_dma(c, st + 3, buf, 1); }
    }
    SBAR(); k1_mfma<SWAP1>(f1, acc); SBAR();
  }
}
__global__ __launch_bounds__(512, 2) void k1_proj(
    const unsigned short* __restrict__ x, const unsigned short* __restrict__ wq, const unsigned short* __restrict__ wk,
    const unsigned short* __restrict__ wv, const unsigned short* __restrict__ wo, const unsigned short* __restrict__ wg,
    const unsigned short* __restrict__ wu, const unsigned short* __restrict__ wd, const unsigned short* __restrict__ gw,
    char* __restrict__ ws) {
  extern __shared__ __attribute__((aligned(16))) char smem[];
  TSDECL; STAMP(0);
  const int tid = threadIdx.x, lane = tid & 63, w = __builtin_amdgcn_readfirstlane(tid >> 6);
  const int hh = lane >> 5, l31 = lane & 31;
  const int bid = blockIdx.x, xcd = bid & 7, idx = bid >> 3;
  unsigned pf1;
  {
    unsigned long long pc; unsigned offk2;
    asm volatile("s_getpc_b64 %0\n.Lcpf%=:\n\ts_mov_b32 %1, _Z8k2_fusedPcPt-.Lcpf%=" : "=s"(pc), "=s"(offk2));
    const unsigned a = (idx * 64 + w * 8 + (lane & 7)) * 64, lim = offk2 < 65536u ? offk2 + 13056 : 0u;
    const char* p = (const char*)pc + (a < lim ? a : lim);
    asm volatile("global_load_dword %0, %1, off" : "=v"(pf1) : "v"(p));
  }
  SBAR();
  const int rb = (xcd & 1) * 8 + (idx & 7), cg = (xcd >> 1) * 4 + ((idx >> 3) ^ 2);
  const int m0 = rb * 128;
  K1Ctx c; c.smem = smem; c.w = w;
  {
    const int krow = 8 * w + ((lane >> 2) & 7), ch = lane & 3, cl = lane >> 5;
    if (cg < 14) {
      c.wp[0] = (const char*)(wg + (size_t)krow * F_ + 128 * cg + 32 * (0 + cl) + 8 * ch);
      c.wp[1] = (const char*)(wg + (size_t)krow * F_ + 128 * cg + 32 * (2 + cl) + 8 * ch);
      c.wp[2] = (const char*)(wu + (size_t)krow * F_ + 128 * cg + 32 * (0 + cl) + 8 * ch);
      c.wp[3] = (const char*)(wu + (size_t)krow * F_ + 128 * cg + 32 * (2 + cl) + 8 * ch);
      c.wstride = 64 * F_ * 2;
    } else {
      const int cb = (cg - 14) * 256;
      c.wp[0] = (const char*)(wq + (size_t)krow * D_ + cb + 32 * (0 + cl) + 8 * ch);
      c.wp[1] = (const char*)(wq + (size_t)krow * D_ + cb + 32 * (2 + cl) + 8 * ch);
      c.wp[2] = (const char*)(wq + (size_t)krow * D_ + cb + 32 * (4 + cl) + 8 * ch);
      if (cg == 14) c.wp[3] = (const char*)(wq + (size_t)krow * D_ + cb + 32 * (6 + cl) + 8 * ch);
      else c.wp[3] = (const char*)((cl ? wv : wk) + (size_t)krow * DH + (ch == 3 ? 20 : 8 * ch));
      c.wstride = 64 * D_ * 2;
    }
    c.wstride3 = (cg == 15) ? 64 * DH * 2 : c.wstride;
    #pragma unroll
    for (int i = 0; i < 2; i++) { const int m = 8 * (2 * w + i) + (lane >> 3); const int ch2 = (lane & 7) ^ ((m >> 1) & 7);
      c.xp[i] = (const char*)(x + (size_t)(m0 + m) * D_ + 8 * ch2); }
  }
  #pragma unroll
  for (int i = 0; i < 6; i++) k1_dma(c, 0, 0, i);
  if (tid < 2) *(volatile LAS unsigned*)(uintptr_t)((unsigned)(uintptr_t)(LAS char*)smem + K1_SYNC + 4 * tid) = 0u;
  asm volatile("s_waitcnt lgkmcnt(0)" ::: "memory");
  SBAR(); BARRIER();
  #pragma unroll
  for (int i = 0; i < 6; i++) k1_dma(c, 1, 1, i);
  SBAR();
  const int gwave = bid * 8 + w;
  const int grow = gwave;
  u32x4 gx, gv; unsigned pt[8];
  const bool isd = gwave < 1568;
  const int pe = isd ? gwave : (gwave < 2016 ? gwave - 1568 : 0);
  const unsigned short *gxp, *gvp, *sp0, *sp1;
  int sslot; bool spad;
  {
    const int l56 = lane < 56 ? lane : 55;
    gxp = x + (size_t)grow * D_ + l56 * 8;
    gvp = gw + l56 * 8;
    const int nt = pe % 14, fs = pe / 14, head = fs >> 1, s = fs & 1;
    sslot = isd ? nt * 112 + fs : (gwave < 2016 ? nt * 32 + fs : gwave - 1568);
    spad = !isd && s;
    const unsigned short* srcb = (isd ? wd : wo) + 32 * nt + l31;
    const int r0 = 16 * s + 4 * hh, r1 = r0 + 8, d1 = r1 < 24 ? r1 : (r1 < 28 ? 0 : r1 - 4);
    sp0 = srcb + (size_t)(isd ? 16 * fs + 4 * hh : head * DH + r0) * D_;
    sp1 = srcb + (size_t)(isd ? 16 * fs + 8 + 4 * hh : head * DH + d1) * D_;
    asm volatile("" : "+v"(gxp), "+v"(gvp), "+v"(sp0), "+v"(sp1));
  }
  auto side = [&]() {
    asm volatile("global_load_dwordx4 %0, %1, off" : "=v"(gx) : "v"(gxp));
    asm volatile("global_load_dwordx4 %0, %1, off" : "=v"(gv) : "v"(gvp));
    #pragma unroll
    for (int j = 0; j < 8; j++) asm volatile("global_load_ushort %0, %1, off offset:%2" : "=v"(pt[j]) : "v"(j < 4 ? sp0 : sp1), "i"((j & 3) * D_ * 2));
  };

  const int wf = w & 3, wm = w >> 2;
  const unsigned lds0 = (unsigned)(uintptr_t)(LAS char*)smem;
  const unsigned abase = lds0 + 4096 * hh + 64 * ((lane & 15) >> 2) + 32 * ((lane >> 4) & 1) + 8 * (lane & 3) + 512 * wf;
  #pragma unroll
  for (int ks = 0; ks < 4; ks++) c.bb[ks] = lds0 + 32768 + 128 * (64 * wm + l31) + 16 * ((2 * ks + hh) ^ ((lane >> 1) & 7));
  c.ab[0] = abase; c.ab[1] = abase + K1_STAGE; c.ab[2] = abase + 2 * K1_STAGE;
  const int head0 = (cg == 15) ? 9 : 0;
  unsigned qoff[2][4];
  #pragma unroll
  for (int a = 0; a < 2; a++)
    #pragma unroll
    for (int g = 0; g < 4; g++) {
      const unsigned G = 8 * ((cg - 14) * 8 + wf + 4 * a) + 2 * g + hh, head = (G * 9363u) >> 16, u = G - 7 * head, pos = (u == 6) ? 7 : u;
      qoff[a][g] = lds0 + K1_STAGE + 2 * wm * 20480 + (head - head0) * 2048 + (pos >> 2) * 1024 + ((pos >> 1) & 1) * 512 + ((pos & 1) * 32 + l31) * 8;
      PIN(qoff[a][g]);
    }
  f32x16 acc[2][2];
  #pragma unroll
  for (int a = 0; a < 2; a++) for (int b = 0; b < 2; b++) for (int r = 0; r < 16; r++) acc[a][b][r] = 0.f;

  auto mid = [&]() {
    asm volatile("" :: "v"(pf1));
    asm volatile("" : "+v"(gx), "+v"(gv), "+v"(pt[0]), "+v"(pt[1]), "+v"(pt[2]), "+v"(pt[3]), "+v"(pt[4]), "+v"(pt[5]), "+v"(pt[6]), "+v"(pt[7]));
    u32x4 o = {(pt[0] & 0xffffu) | (pt[1] << 16), (pt[2] & 0xffffu) | (pt[3] << 16), (pt[4] & 0xffffu) | (pt[5] << 16), (pt[6] & 0xffffu) | (pt[7] << 16)};
    if (spad && hh == 0) { o[2] = 0u; o[3] = 0u; }
    char* dst = ws + (isd ? WS_WD : WS_WO) + (size_t)sslot * 1024;
        asm volatile("global_store_dwordx4 %0, %1, off sc1\n s_nop 1" ::"v"(dst + lane * 16), "v"(o) : "memory");
    float sum = 0.f;
    #pragma unroll
    for (int i = 0; i < 4; i++) {
      sum += __uint_as_float(gx[i] << 16) * __uint_as_float(gv[i] << 16);
      sum += __uint_as_float(gx[i] & 0xffff0000u) * __uint_as_float(gv[i] & 0xffff0000u);
    }
    if (lane >= 56) sum = 0.f;
    sum = dpp_sum64(sum);
    const float gte = 1.f / (1.f + __expf(-sum));
    float* gp = (float*)(ws + WS_G) + grow;
    if (lane == 63) asm volatile("global_store_dword %0, %1, off" ::"v"(gp), "v"(gte) : "memory");
  };

  WAITVM(6);
  BARRIER(); STAMP(1);
  const bool vwave = (cg == 15) && (wf == 3);
  if (vwave) k1_main<true>(c, acc, mid, side); else k1_main<false>(c, acc, mid, side);
  STAMP(2);

  if (cg < 14) {
    #pragma unroll
    for (int b = 0; b < 2; b++) {
      const int tt = 4 * rb + 2 * wm + b, ft = 4 * cg + wf;
      #pragma unroll
      for (int s = 0; s < 2; s++) {
        u32x4 o;
        #pragma unroll
        for (int j = 0; j < 4; j++) {
          const int r = 8 * s + 2 * j;
          o[j] = cvtpk(acc[0][b][r] * acc[1][b][r], acc[0][b][r + 1] * acc[1][b][r + 1]);
        }
        st16(ws + WS_H + ((size_t)(tt * 112 + 2 * ft + s) * 64 + lane) * 16, o);
      }
    }
  } else {
    const float qs = 0.18898223650461361f * 1.4426950408889634f;
    const bool kvw = (cg == 15) && (wf >= 2);
    auto putq = [&](const f32x16& v, unsigned ad) {
      #pragma unroll
      for (int g = 0; g < 4; g++)
        *(LAS u32x2*)(uintptr_t)(qoff[ad & 1][g] + (ad >> 1) * 20480) = (u32x2){cvtpk(v[4 * g], v[4 * g + 1]), cvtpk(v[4 * g + 2], v[4 * g + 3])};
    };
    putq(acc[0][0], 0); putq(acc[0][1], 2);
    if (!kvw) { putq(acc[1][0], 1); putq(acc[1][1], 3); }
    else {
      const bool isk = wf == 2, vpad = !isk && l31 >= 24 && l31 < 28, kpad = isk && hh == 0;
      const unsigned vf = (l31 == 24) ? 0x3f803f80u : 0u;
      const float sc = isk ? qs : 1.f;
      char* kvp = ws + WS_KV + (size_t)(4 * rb + 2 * wm) * 4096 + (wf - 2) * 2048 + lane * 16;
      #pragma unroll
      for (int b = 0; b < 2; b++)
        #pragma unroll
        for (int s = 0; s < 2; s++) {
          u32x4 o;
          #pragma unroll
          for (int j = 0; j < 4; j++) { o[j] = cvtpk(acc[1][b][8 * s + 2 * j] * sc, acc[1][b][8 * s + 2 * j + 1] * sc);
            if (s == 1 && j >= 2) o[j] = kpad ? 0u : o[j];
            o[j] = vpad ? vf : o[j]; }
          st16(kvp + b * 4096 + s * 1024, o);
        }
    }
    {
      volatile LAS unsigned* ca = (volatile LAS unsigned*)(uintptr_t)(lds0 + K1_SYNC + 4 * wm);
      asm volatile("s_waitcnt lgkmcnt(0)" ::: "memory");
      if (lane == 0) asm volatile("ds_add_u32 %0, %1" ::"v"(lds0 + K1_SYNC + 4 * wm), "v"(1u) : "memory");
      while ((unsigned)__builtin_amdgcn_readfirstlane(*ca) < 4u) __builtin_amdgcn_s_sleep(1);
      asm volatile("" ::: "memory");
    }
    const int tl = w >> 1, hs0 = (cg == 14) ? 9 * (w & 1) : 1 + 6 * (w & 1);
    const char* ip = smem + K1_STAGE + tl * 20480 + hs0 * 1024 + lane * 8;
    char* qg = ws + WS_Q + (size_t)(4 * rb + tl) * 32768 + (head0 * 2 + hs0) * 1024 + lane * 16;
    u32x2 lo[9], hi[9];
    #pragma unroll
    for (int i = 0; i < 9; i++) { lo[i] = *(const u32x2*)(ip + i * 1024); hi[i] = *(const u32x2*)(ip + i * 1024 + 512); }
    const char* pp = smem + K1_STAGE + tl * 20480 + (9 - head0) * 2048 + lane * 8;
    const u32x2 l2 = *(const u32x2*)pp, h2 = *(const u32x2*)(pp + 512);
    #pragma unroll
    for (int i = 0; i < 7; i++) st16(qg + i * 1024, (u32x4){lo[i][0], lo[i][1], hi[i][0], hi[i][1]});
    char* d = ws + WS_Q + (size_t)(4 * rb + tl) * 32768 + 9 * 2048 + lane * 16;
    if (cg == 14) {
      st16(qg + 7 * 1024, (u32x4){lo[7][0], lo[7][1], hi[7][0], hi[7][1]});
      st16(qg + 8 * 1024, (u32x4){lo[8][0], lo[8][1], hi[8][0], hi[8][1]});
      if (hh == 0 && (w & 1)) *(u32x2*)d = l2;
    } else if (w & 1) {
      if (hh) *(u32x2*)d = l2;
      *(u32x2*)(d + 8) = h2;
    }
  }
  if (bid < 128 && tid == 0) *(unsigned*)(ws + WS_CNT + bid * 256) = 0u;
  STAMP(3); TSFLUSH(0);
}

typedef __amdgpu_buffer_rsrc_t rsrc_t;
#define BLD(r, vo, so, imm) __builtin_amdgcn_raw_buffer_load_b128(r, (vo) + (imm), so, 0)
#define MF_S0(n, k, q) asm volatile("v_mfma_f32_32x32x16_bf16 %0, %1, %2, 0" : "=&v"(n) : "v"(k), "a"(q))
#define MF_S1(n, k, q) asm volatile("v_mfma_f32_32x32x16_bf16 %0, %1, %2, %0" : "+v"(n) : "v"(k), "a"(q))
#define MF_O(o, v, p) asm volatile("v_mfma_f32_32x32x16_bf16 %0, %1, %2, %0" : "+a"(o) : "v"(v), "v"(p))
#define MF_DV(d, a, b) asm volatile("v_mfma_f32_32x32x16_bf16 %0, %1, %2, %0" : "+a"(d) : "v"(a), "v"(b))
#define MF_DA(d, a, b) asm volatile("v_mfma_f32_32x32x16_bf16 %0, %1, %2, %0" : "+a"(d) : "a"(a), "a"(b))
#define EXP2(x) __builtin_amdgcn_exp2f(x)
constexpr int K2_LDS = 16 * 4352;
struct KVF { u32x4 k0, k1, v0, v1; };
struct DKF { u32x4 a0, a1, b0, b1; };
__device__ __forceinline__ unsigned ptrunc(float lo, float hi) { return __builtin_amdgcn_perm(__float_as_uint(hi), __float_as_uint(lo), 0x07060302u); }
struct AttnState {
  f32x16 S[2], O[4], D[2][2];
  u32x4 q[4][2], P1; float e14, e15;
  KVF kv[4]; DKF dk[8];
  rsrc_t rW, rLo, rHi; unsigned vKV, vA0, vA1, vB0, vB1;
  u32x4 wo[2][8]; rsrc_t rJ; unsigned vWO, vG, ldw; float gq, g0, g1;
};
constexpr int RSLOT = 4 * 1088, RTILE = 4 * RSLOT;
template <int TILE, int G> __device__ __forceinline__ void put_a(unsigned addr, const f32x16& v) {
  *(volatile LAS f32x4*)(uintptr_t)(addr + TILE * RTILE + G * 1088) = __builtin_shufflevector(v, v, 4 * G, 4 * G + 1, 4 * G + 2, 4 * G + 3);
}
template <int X, int J, int GAP> __device__ __forceinline__ void extra(AttnState& t) {
  if (X == 1) {
    constexpr int n = (GAP == 1) ? 0 : (GAP == 3) ? 1 : (GAP == 4) ? 2 : (GAP == 6) ? 3 : -1;
    if (n >= 0) { constexpr int k = 2 * J + (n >> 1), a = n & 1; t.wo[a][k & 7] = BLD(t.rJ, t.vWO + a * 32768, (k >> 2) * 4096, (k & 3) * 1024); }
    if (GAP == 5 && J == 0) t.gq = __builtin_bit_cast(float, __builtin_amdgcn_raw_buffer_load_b32(t.rW, t.vG, 0, 0));
  }
  if (X == 2) {
    if (J == 2 && GAP >= 2 && GAP <= 5) put_a<0, (GAP - 2) & 3>(t.ldw, t.O[0]);
    if (J == 3 && GAP >= 1 && GAP <= 4) put_a<1, (GAP - 1) & 3>(t.ldw, t.O[1]);
    if (J == 3 && GAP == 6) { put_a<2, 0>(t.ldw, t.O[2]); put_a<2, 1>(t.ldw, t.O[2]); put_a<2, 2>(t.ldw, t.O[2]); put_a<2, 3>(t.ldw, t.O[2]); }
  }
}
template <int SLOT> __device__ __forceinline__ void mf_d(f32x16& d, const u32x4& a, const u32x4& b) { if (SLOT < 4) MF_DV(d, a, b); else MF_DA(d, a, b); }
template <int SLOT, int PC> __device__ __forceinline__ void dk_ld(AttnState& t, rsrc_t r, unsigned so) {
  constexpr int imm = (SLOT & 3) * 1024;
  if (PC == 0) t.dk[SLOT].b1 = BLD(r, t.vB1, so, imm);
  if (PC == 1) t.dk[SLOT].a1 = BLD(r, t.vA1, so, imm);
  if (PC == 2) t.dk[SLOT].b0 = BLD(r, t.vB0, so, imm);
  if (PC == 3) t.dk[SLOT].a0 = BLD(r, t.vA0, so, imm);
}
template <int SLOT, int PC> __device__ __forceinline__ void kv_ld(AttnState& t, unsigned so) {
  if (PC == 0) t.kv[SLOT].v1 = BLD(t.rW, t.vKV, so, 3072);
  if (PC == 1) t.kv[SLOT].v0 = BLD(t.rW, t.vKV, so, 2048);
  if (PC == 2) t.kv[SLOT].k1 = BLD(t.rW, t.vKV, so, 1024);
  if (PC == 3) t.kv[SLOT].k0 = BLD(t.rW, t.vKV, so, 0);
}
template <int U, int J, bool DOWN, bool NEXT, bool KVL, bool DLA, bool DLB, int X>
__device__ __forceinline__ void attn_step(AttnState& t, unsigned sokv, unsigned sodA, unsigned sodB) {
  constexpr int PU = (U + 3) & 3, NU = (U + 1) & 3;
  constexpr int DS = 2 * U + (J >> 1);
  constexpr int RS = (J < 2) ? ((2 * U + 7) & 7) : 2 * U;
  constexpr bool DL = (J < 2) ? DLA : DLB;
  f32x16& Sc = t.S[J & 1]; f32x16& Sn = t.S[(J + 1) & 1];
  const KVF& kc = t.kv[U]; const KVF& kk = (J == 3) ? t.kv[NU] : t.kv[U];
  const unsigned sod = (J < 2) ? sodA : sodB;
  const rsrc_t rd = (RS >= 4 && RS < 7) ? t.rHi : t.rLo;
  float e0, e1, e2, e3, e4, e5, e6, e7, e8, e9, e10, e11, e12, e13; u32x4 P0;
  if (NEXT) MF_S0(Sn, kk.k0, t.q[(J + 1) & 3][0]);
  SBAR();
  t.P1[3] = ptrunc(t.e14, t.e15); SBAR();
  e0 = EXP2(Sc[0]); e1 = EXP2(Sc[1]); e2 = EXP2(Sc[2]);
  extra<X, J, 1>(t);
  SBAR();
  if (J == 0) MF_O(t.O[3], t.kv[PU].v1, t.P1); else MF_O(t.O[J - 1], kc.v1, t.P1);
  SBAR();
  e3 = EXP2(Sc[3]); e4 = EXP2(Sc[4]); e5 = EXP2(Sc[5]); P0[0] = ptrunc(e0, e1);
  extra<X, J, 2>(t);
  SBAR();
  if (NEXT) MF_S1(Sn, kk.k1, t.q[(J + 1) & 3][1]);
  SBAR();
  e6 = EXP2(Sc[6]); e7 = EXP2(Sc[7]); P0[1] = ptrunc(e2, e3); P0[2] = ptrunc(e4, e5);
  if (KVL && J < 2) kv_ld<PU, 2 * J>(t, sokv);
  if (DL && J >= 2) dk_ld<RS, 2 * (J & 1)>(t, rd, sod);
  extra<X, J, 3>(t);
  SBAR();
  if (DOWN) { if (J & 1) mf_d<DS>(t.D[0][1], t.dk[DS].a0, t.dk[DS].b1); else mf_d<DS>(t.D[0][0], t.dk[DS].a0, t.dk[DS].b0); }
  SBAR();
  P0[3] = ptrunc(e6, e7); SBAR();
  e8 = EXP2(Sc[8]); e9 = EXP2(Sc[9]); e10 = EXP2(Sc[10]);
  if (DL && J < 2) dk_ld<RS, 2 * (J & 1)>(t, rd, sod);
  extra<X, J, 4>(t);
  SBAR();
  MF_O(t.O[J], kc.v0, P0);
  SBAR();
  e11 = EXP2(Sc[11]); e12 = EXP2(Sc[12]); e13 = EXP2(Sc[13]); t.P1[0] = ptrunc(e8, e9);
  if (DL && J < 2) dk_ld<RS, 2 * (J & 1) + 1>(t, rd, sod);
  extra<X, J, 5>(t);
  SBAR();
  if (DOWN) { if (J & 1) mf_d<DS>(t.D[1][1], t.dk[DS].a1, t.dk[DS].b1); else mf_d<DS>(t.D[1][0], t.dk[DS].a1, t.dk[DS].b0); }
  SBAR();
  t.e14 = EXP2(Sc[14]); t.e15 = EXP2(Sc[15]); t.P1[1] = ptrunc(e10, e11); t.P1[2] = ptrunc(e12, e13);
  if (KVL && J < 2) kv_ld<PU, 2 * J + 1>(t, sokv);
  if (DL && J >= 2) dk_ld<RS, 2 * (J & 1) + 1>(t, rd, sod);
  extra<X, J, 6>(t);
  SBAR();
}
template <int U, bool DOWN, bool LAST, bool KVL, bool DLA, bool DLB, int X = 0>
__device__ __forceinline__ void attn_tile(AttnState& t, unsigned sokv, unsigned sodA, unsigned sodB) {
  attn_step<U, 0, DOWN, true, KVL, DLA, DLB, X>(t, sokv, sodA, sodB);
  attn_step<U, 1, DOWN, true, KVL, DLA, DLB, X>(t, sokv, sodA, sodB);
  attn_step<U, 2, DOWN, true, KVL, DLA, DLB, X>(t, sokv, sodA, sodB);
  attn_step<U, 3, DOWN, !LAST, KVL, DLA, DLB, X>(t, sokv, sodA, sodB);
}
__global__ __launch_bounds__(256, 1) void k2_fused(char* __restrict__ ws, unsigned short* __restrict__ out) {
  extern __shared__ __attribute__((aligned(16))) char smem[];
  TSDECL; STAMP(0);
  const int tid = threadIdx.x, lane = tid & 63, kq = __builtin_amdgcn_readfirstlane(tid >> 6);
  const int hh = lane >> 5, l31 = lane & 31;
  const int bid = blockIdx.x, xcd = bid & 7, idx = bid >> 3;
  const int ttile = xcd * 8 + (idx >> 2), qt0 = ttile * 16 + 4 * (idx & 3);
  const bool job = idx < 28;
  const int tp = xcd * 4 + (idx & 3), np = job ? (idx >> 2) : 0;
  constexpr int WSN = 1 << 25;
  const rsrc_t rW = __builtin_amdgcn_make_buffer_rsrc(ws, 0, WSN, 0x00020000);
  const rsrc_t rJ = __builtin_amdgcn_make_buffer_rsrc(ws, 0, job ? WSN : 0, 0x00020000);
  AttnState t;
  t.rW = rW; t.rLo = rJ; t.rHi = rJ; t.rJ = rJ;
  t.vWO = (unsigned)WS_WO + ((2 * np) * 32 + 8 * kq) * 1024 + lane * 16;
  t.vG = (unsigned)WS_G + (ttile * 32 + l31) * 4;
  t.ldw = (unsigned)(uintptr_t)(LAS char*)smem + kq * RSLOT + lane * 16;
  t.vKV = (unsigned)WS_KV + kq * 65536 + lane * 16;
  t.vA0 = (unsigned)WS_WD + ((2 * np) * 112 + 28 * kq) * 1024 + lane * 16; t.vA1 = t.vA0 + 112 * 1024;
  t.vB0 = (unsigned)WS_H + ((2 * tp) * 112 + 28 * kq) * 1024 + lane * 16; t.vB1 = t.vB0 + 112 * 1024;
  {
    const unsigned vq = (unsigned)WS_Q + qt0 * 2048 + lane * 16;
    kv_ld<0, 3>(t, 0); kv_ld<0, 2>(t, 0);
    u32x4 q1[4];
    #pragma unroll
    for (int j = 0; j < 4; j++) { t.q[j][0] = BLD(rW, vq, j * 2048, 0); q1[j] = BLD(rW, vq, j * 2048, 1024); }
    kv_ld<0, 1>(t, 0); kv_ld<0, 0>(t, 0);
    SBAR();
#define DKG(S_, SO) dk_ld<S_, 0>(t, rJ, SO); SBAR(); dk_ld<S_, 1>(t, rJ, SO); SBAR(); dk_ld<S_, 2>(t, rJ, SO); SBAR(); dk_ld<S_, 3>(t, rJ, SO); SBAR()
#define KVG(S_, SO) kv_ld<S_, 0>(t, SO); SBAR(); kv_ld<S_, 1>(t, SO); SBAR(); kv_ld<S_, 2>(t, SO); SBAR(); kv_ld<S_, 3>(t, SO); SBAR()
    DKG(0, 0); KVG(1, 4096); DKG(1, 0); DKG(2, 0); KVG(2, 8192); DKG(3, 0); DKG(4, 4096); DKG(5, 4096); DKG(6, 4096);
    #pragma unroll
    for (int j = 0; j < 4; j++)
      #pragma unroll
      for (int r = 0; r < 16; r++) { t.O[j][r] = 0.f; t.D[j >> 1][j & 1][r] = 0.f; }
    asm volatile("" : "+a"(t.O[0]), "+a"(t.O[1]), "+a"(t.O[2]), "+a"(t.O[3]), "+a"(t.D[0][0]), "+a"(t.D[0][1]), "+a"(t.D[1][0]), "+a"(t.D[1][1]));
    SBAR();
    #pragma unroll
    for (int j = 0; j < 4; j++) {
      if (hh == 0) { q1[j][2] = 0u; q1[j][3] = 0u; }
      t.q[j][1] = q1[j];
    }
    t.P1 = (u32x4){0u, 0u, 0u, 0u}; t.e14 = 0.f; t.e15 = 0.f; t.kv[3].v1 = (u32x4){0u, 0u, 0u, 0u};
    asm volatile("s_nop 1" : "+a"(t.q[0][0]), "+a"(t.q[0][1]), "+a"(t.q[1][1]), "+a"(t.q[2][1]), "+a"(t.q[3][1]), "+a"(t.O[0]), "+a"(t.O[1]), "+a"(t.O[2]), "+a"(t.O[3]),
                 "+a"(t.D[0][0]), "+a"(t.D[0][1]), "+a"(t.D[1][0]), "+a"(t.D[1][1]), "+v"(t.kv[3].v1));
    MF_S0(t.S[0], t.kv[0].k0, t.q[0][0]); MF_S1(t.S[0], t.kv[0].k1, t.q[0][1]);
    asm volatile("s_nop 15" : "+v"(t.S[0]));
    SBAR();
  }
  STAMP(1);
  #pragma unroll 1
  for (int i = 0; i < 3; i++) {
    const unsigned sk = i * 16384, sd = i * 8192;
    t.rHi = __builtin_amdgcn_make_buffer_rsrc(ws, 0, (job && i < 2) ? WSN : 0, 0x00020000);
    attn_tile<0, true, false, true, true, true>(t, sk + 3 * 4096, sd + 4096, sd + 8192);
    attn_tile<1, true, false, true, true, true>(t, sk + 4 * 4096, sd + 8192, sd + 8192);
    attn_tile<2, true, false, true, true, true>(t, sk + 5 * 4096, sd + 8192, sd + 8192 + 4096);
    attn_tile<3, true, false, true, true, true>(t, sk + 6 * 4096, sd + 8192 + 4096, sd + 8192 + 4096);
  }
  attn_tile<0, true, false, true, false, false>(t, 15 * 4096, 0, 0);
  attn_tile<1, true, false, false, false, false>(t, 0, 0, 0);
  attn_tile<2, false, false, false, false, false, 1>(t, 0, 0, 0);
  t.g0 = __builtin_bit_cast(float, __builtin_amdgcn_raw_buffer_load_b32(rW, (unsigned)WS_G + (2 * tp * 32 + l31) * 4, 0, 0));
  t.g1 = __builtin_bit_cast(float, __builtin_amdgcn_raw_buffer_load_b32(rW, (unsigned)WS_G + (2 * tp * 32 + 32 + l31) * 4, 0, 0));
  SBAR();
  attn_tile<3, false, true, false, false, false, 2>(t, 0, 0, 0);
  t.P1[3] = ptrunc(t.e14, t.e15);
  asm volatile("s_nop 1" : "+v"(t.P1));
  MF_O(t.O[3], t.kv[3].v1, t.P1);
  asm volatile("s_nop 15" : "+a"(t.O[3]));
  put_a<3, 0>(t.ldw, t.O[3]); put_a<3, 1>(t.ldw, t.O[3]); put_a<3, 2>(t.ldw, t.O[3]); put_a<3, 3>(t.ldw, t.O[3]);
  STAMP(2);
  asm volatile("s_waitcnt lgkmcnt(0)\n s_barrier" ::: "memory");
  f32x16 o;
  {
    const char* rp = smem + kq * RTILE + lane * 16;
    f32x4 pv[4][4];
    #pragma unroll
    for (int w = 0; w < 4; w++)
      #pragma unroll
      for (int g = 0; g < 4; g++) pv[w][g] = *(const f32x4*)(rp + w * RSLOT + g * 1088);
    #pragma unroll
    for (int g = 0; g < 4; g++) { const f32x4 v = (pv[0][g] + pv[1][g]) + (pv[2][g] + pv[3][g]);
      o[4 * g] = v[0]; o[4 * g + 1] = v[1]; o[4 * g + 2] = v[2]; o[4 * g + 3] = v[3]; }
  }
  const int qt = qt0 + kq;
  {
    float l = __shfl(o[12], lane & 31);
    bool bad = !(l >= 8.673617379884035e-19f && l <= 1.152921504606847e18f);
    #pragma unroll
    for (int r = 0; r < 16; r++) bad |= !(__builtin_fabsf(o[r]) <= 3.0e38f);
    if (__builtin_expect(__any(bad), 0)) {
      const char* kva = ws + WS_KV + lane * 16;
      const bf16x8 a0 = *(const bf16x8*)(ws + WS_Q + (size_t)qt * 2048 + lane * 16);
      u32x4 t1 = *(const u32x4*)(ws + WS_Q + (size_t)qt * 2048 + 1024 + lane * 16); if (hh == 0) { t1[2] = 0u; t1[3] = 0u; }
      const bf16x8 a1 = __builtin_bit_cast(bf16x8, t1);
      const f32x16 Z = {0, 0, 0, 0, 0, 0, 0, 0, 0, 0, 0, 0, 0, 0, 0, 0};
      float m = -__builtin_inff();
      #pragma unroll 1
      for (int k = 0; k < 64; k++) {
        const char* p = kva + (size_t)k * 4096;
        f32x16 Sx = MFMA(*(const bf16x8*)p, a0, Z); Sx = MFMA(*(const bf16x8*)(p + 1024), a1, Sx);
        #pragma unroll
        for (int r = 0; r < 16; r++) m = __builtin_fmaxf(m, Sx[r]);
      }
      m = __builtin_fmaxf(m, __shfl_xor(m, 32));
      o = Z;
      #pragma unroll 1
      for (int k = 0; k < 64; k++) {
        const char* p = kva + (size_t)k * 4096;
        f32x16 Sx = MFMA(*(const bf16x8*)p, a0, Z); Sx = MFMA(*(const bf16x8*)(p + 1024), a1, Sx);
        u32x4 p0, p1;
        #pragma unroll
        for (int j = 0; j < 4; j++) {
          p0[j] = cvtpk(EXP2(Sx[2 * j] - m), EXP2(Sx[2 * j + 1] - m));
          p1[j] = cvtpk(EXP2(Sx[8 + 2 * j] - m), EXP2(Sx[8 + 2 * j + 1] - m));
        }
        o = MFMA(*(const bf16x8*)(p + 2048), __builtin_bit_cast(bf16x8, p0), o);
        o = MFMA(*(const bf16x8*)(p + 3072), __builtin_bit_cast(bf16x8, p1), o);
      }
      l = __shfl(o[12], lane & 31);
    }
    const float inv = t.gq / l;
    u32x4 o0, o1;
    #pragma unroll
    for (int j = 0; j < 4; j++) { o0[j] = cvtpk(o[2 * j] * inv, o[2 * j + 1] * inv); o1[j] = cvtpk(o[8 + 2 * j] * inv, o[8 + 2 * j + 1] * inv); }
    if (hh == 0) { o1[2] = 0u; o1[3] = 0u; }
    const unsigned vo = (unsigned)WS_O + qt * 2048 + lane * 16;
    __builtin_amdgcn_raw_buffer_store_b128(o0, rW, vo, 0, 16);
    __builtin_amdgcn_raw_buffer_store_b128(o1, rW, vo + 1024, 0, 16);
  }
  SBAR();
  f32x16 acc[2][2];
  asm volatile("" : "+a"(t.D[0][0]), "+a"(t.D[0][1]), "+a"(t.D[1][0]), "+a"(t.D[1][1]));
  {
    const float og0 = 1.f - t.g0, og1 = 1.f - t.g1;
    #pragma unroll
    for (int a = 0; a < 2; a++) { acc[a][0] = t.D[a][0] * og0; acc[a][1] = t.D[a][1] * og1; }
  }
  asm volatile("" : "+v"(acc[0][0]), "+v"(acc[0][1]), "+v"(acc[1][0]), "+v"(acc[1][1]));
  asm volatile("s_waitcnt vmcnt(0)\n s_barrier" ::: "memory");
  unsigned* cnt = (unsigned*)(ws + WS_CNT);
  if (tid == 0) __hip_atomic_fetch_add(cnt + ((ttile >> 1) * 4 + (idx & 3)) * 64, 1u, __ATOMIC_RELAXED, __HIP_MEMORY_SCOPE_AGENT);
  STAMP(3);
  if (!job) { TSFLUSH(1); return; }
  for (unsigned spins = 0; spins < (1u << 22); spins++) {
    if ((unsigned)__builtin_amdgcn_readfirstlane(__hip_atomic_load(cnt + (tp * 4 + kq) * 64, __ATOMIC_RELAXED, __HIP_MEMORY_SCOPE_AGENT)) >= 2u) break;
    __builtin_amdgcn_s_sleep(1);
  }
  asm volatile("" ::: "memory");
  {
    const unsigned vo = (unsigned)WS_O + ((2 * tp) * 32 + 8 * kq) * 1024 + lane * 16;
    u32x4 ob[2][8];
    #pragma unroll
    for (int k = 0; k < 8; k++) {
      ob[0][k] = __builtin_amdgcn_raw_buffer_load_b128(rW, vo + (k & 3) * 1024, (k >> 2) * 4096, 16);
      ob[1][k] = __builtin_amdgcn_raw_buffer_load_b128(rW, vo + 32768 + (k & 3) * 1024, (k >> 2) * 4096, 16);
    }
    SBAR();
    #pragma unroll
    for (int k = 0; k < 8; k++)
      #pragma unroll
      for (int a = 0; a < 2; a++)
        #pragma unroll
        for (int b = 0; b < 2; b++) acc[a][b] = MFMA(__builtin_bit_cast(bf16x8, t.wo[a][k]), __builtin_bit_cast(bf16x8, ob[b][k]), acc[a][b]);
  }
  put_a<0, 0>(t.ldw, acc[0][0]); put_a<0, 1>(t.ldw, acc[0][0]); put_a<0, 2>(t.ldw, acc[0][0]); put_a<0, 3>(t.ldw, acc[0][0]);
  put_a<1, 0>(t.ldw, acc[0][1]); put_a<1, 1>(t.ldw, acc[0][1]); put_a<1, 2>(t.ldw, acc[0][1]); put_a<1, 3>(t.ldw, acc[0][1]);
  put_a<2, 0>(t.ldw, acc[1][0]); put_a<2, 1>(t.ldw, acc[1][0]); put_a<2, 2>(t.ldw, acc[1][0]); put_a<2, 3>(t.ldw, acc[1][0]);
  put_a<3, 0>(t.ldw, acc[1][1]); put_a<3, 1>(t.ldw, acc[1][1]); put_a<3, 2>(t.ldw, acc[1][1]); put_a<3, 3>(t.ldw, acc[1][1]);
  asm volatile("s_waitcnt lgkmcnt(0)\n s_barrier" ::: "memory");
  {
    const int b = kq >> 1, c = lane & 7, a = c >> 2, g = c & 3;
    #pragma unroll
    for (int i = 0; i < 2; i++) {
      const int tk = 16 * (kq & 1) + 8 * i + (lane >> 3);
      const char* rp = smem + (2 * a + b) * RTILE + g * 1088 + tk * 16;
      f32x4 lo[4], hi[4];
      #pragma unroll
      for (int w = 0; w < 4; w++) { lo[w] = *(const f32x4*)(rp + w * RSLOT); hi[w] = *(const f32x4*)(rp + w * RSLOT + 512); }
      const f32x4 l4 = (lo[0] + lo[1]) + (lo[2] + lo[3]), h4 = (hi[0] + hi[1]) + (hi[2] + hi[3]);
      const u32x4 ov = {cvtpk(l4[0], l4[1]), cvtpk(l4[2], l4[3]), cvtpk(h4[0], h4[1]), cvtpk(h4[2], h4[3])};
      unsigned short* op = out + (size_t)((2 * tp + b) * 32 + tk) * D_ + np * 64 + 8 * c;
      asm volatile("global_store_dwordx4 %0, %1, off sc1\n s_nop 1" ::"v"(op), "v"(ov) : "memory");
    }
  }
  TSFLUSH(1);
}

extern "C" void kernel_launch(void* const* d_in, const int* in_sizes, int n_in, void* d_out, int out_size, void* d_ws, size_t ws_size, hipStream_t stream) {
  const unsigned short* x = (const unsigned short*)d_in[0];
  const unsigned short* wq = (const unsigned short*)d_in[1];
  const unsigned short* wk = (const unsigned short*)d_in[2];
  const unsigned short* wv = (const unsigned short*)d_in[3];
  const unsigned short* wo = (const unsigned short*)d_in[4];
  const unsigned short* wg = (const unsigned short*)d_in[6];
  const unsigned short* wu = (const unsigned short*)d_in[7];
  const unsigned short* wd = (const unsigned short*)d_in[8];
  const unsigned short* gw = (const unsigned short*)d_in[9];
  char* ws = (char*)d_ws;
  (void)hipFuncSetAttribute((const void*)k1_proj, hipFuncAttributeMaxDynamicSharedMemorySize, K1_LDS);
  (void)hipFuncSetAttribute((const void*)k2_fused, hipFuncAttributeMaxDynamicSharedMemorySize, K2_LDS);
  k1_proj<<<256, 512, K1_LDS, stream>>>(x, wq, wk, wv, wo, wg, wu, wd, gw, ws);
  k2_fused<<<256, 256, K2_LDS, stream>>>(ws, (unsigned short*)d_out);
}
```
